```python
import torch

def _softmax(x, axis=-1):
    return torch.softmax(x, dim=axis)
def _gelu(x):
    return torch.nn.functional.gelu(x, approximate="tanh")
def _silu(x):
    return torch.nn.functional.silu(x)
def _sigmoid(x):
    return torch.sigmoid(x)
def _layernorm(x, axis=-1, eps=1e-5):
    m = x.mean(dim=axis, keepdim=True)
    v = x.var(dim=axis, keepdim=True, unbiased=False)
    return (x - m) / torch.sqrt(v + eps)
def _scatter_add(tgt, idx, src, axis=0):
    return tgt.clone().index_add_(axis, idx, src)
def _segment_sum(src, seg, n):
    out = torch.zeros((n,) + src.shape[1:], dtype=src.dtype)
    return out.index_add_(0, seg, src)
def _pad(x, pw):
    flat = tuple(v for pair in reversed(pw) for v in pair)
    return torch.nn.functional.pad(x, flat)

kernel_name = 'standalone__paged_block_table_kv_pagedattention__sampling'

def setup_inputs():

    _w = (lambda a: (lambda t: t.to(torch.bfloat16) if t.is_floating_point() else t)(torch.as_tensor(a)))
    B, Hq, Hkv, Sq, Skv, D = 16, 32, 8, 16, 4096, 128
    bs = 16
    nbps = max(1, Skv // bs)
    num_phys = B * nbps + 4
    bt = torch.randperm(num_phys)[: B * nbps].reshape(B, nbps).to(torch.int64)
    cl = (nbps * bs - (torch.arange(1, B + 1, dtype=torch.int64) * 2654435761) % max(1, nbps * bs // 2)).to(torch.int64)
    return {
        "q": _w((lambda *s: torch.randn(*s, dtype=torch.bfloat16) * 0.8)(B, Hq, Sq, D)),
        "k_cache": _w((lambda *s: torch.randn(*s, dtype=torch.bfloat16) * 0.8)(num_phys, bs, Hkv, D)),
        "v_cache": _w((lambda *s: torch.randn(*s, dtype=torch.bfloat16) * 0.05)(num_phys, bs, Hkv, D)),
        "block_table": _w(bt),
        "context_lens": _w(cl),
    }

def reference(q, k_cache, v_cache, block_table, context_lens):
    B, Hq, Sq, D = q.shape
    bs = k_cache.shape[1]
    Hkv = k_cache.shape[2]
    nbps = block_table.shape[1]
    Skv = nbps * bs
    bt = block_table.reshape(-1)
    k = (lambda x, *d: x.permute(*d))(k_cache[bt].reshape(B, Skv, Hkv, D), 0, 2, 1, 3)
    v = (lambda x, *d: x.permute(*d))(v_cache[bt].reshape(B, Skv, Hkv, D), 0, 2, 1, 3)
    g = Hq // Hkv
    qg = q.reshape(B, Hkv, g, Sq, D)
    scores = torch.einsum("bhgqd,bhkd->bhgqk", qg, k) * (D ** -0.5)
    pos = torch.arange(Skv)
    msk = pos[None, :] < context_lens[:, None]
    scores = torch.where(msk[:, None, None, None, :], scores, -1e9)
    p = _softmax(scores, axis=-1)
    out = torch.einsum("bhgqk,bhkd->bhgqd", p, v)
    return out.reshape(B, Hq, Sq, D)
```

```cpp
#include <hip/hip_runtime.h>
#include <stdint.h>

#define KV_NT 1
#define XSLACK 10
#define XMAP 0

#define TAKE(k) do {} while (0)
#define FLUSH_STAMPS() do {} while (0)

namespace {

typedef __attribute__((ext_vector_type(8))) __bf16 bf16x8;
typedef __attribute__((ext_vector_type(4))) __bf16 bf16x4;
typedef __attribute__((ext_vector_type(2))) __bf16 bf16x2;
typedef __attribute__((ext_vector_type(4))) float f32x4;
typedef __attribute__((ext_vector_type(2))) float f32x2;
typedef __attribute__((ext_vector_type(4))) unsigned u32x4;
typedef __attribute__((ext_vector_type(2))) unsigned u32x2;
typedef unsigned long long u64;
#define LAS __attribute__((address_space(3)))
#define GAS __attribute__((address_space(1)))

constexpr int NB = 16, HQ = 32, HKV = 8, SQ = 16, HD = 128, NBPS = 256, SKV = 4096;
constexpr int NS = 4;
constexpr unsigned SLOT = 32768, VOFF = 16384, QOFF = NS * SLOT, LDS_BYTES = QOFF + 16384;
constexpr unsigned REC_STRIDE = 128;
constexpr unsigned PO_OFF = 65536, PO_WAVE = 9 * 1024, PO_SLOT = 4 * PO_WAVE, PO_BYTES = 256 * PO_SLOT;
constexpr u64 TAG_STEP = 0x9E3779B97F4A7C15ull;
constexpr float CSCALE = 0.08838834764831845f * 1.4426950408889634f;
constexpr int I_FIRST = 0x100, I_UNI = 0x200;

__device__ __forceinline__ int rl(int v, int l) { return __builtin_amdgcn_readlane(v, l); }
__device__ __forceinline__ int scan16(int x) {
  x += __builtin_amdgcn_update_dpp(0, x, 0x111, 0xf, 0xf, false);
  x += __builtin_amdgcn_update_dpp(0, x, 0x112, 0xf, 0xf, false);
  x += __builtin_amdgcn_update_dpp(0, x, 0x114, 0xf, 0xf, false);
  x += __builtin_amdgcn_update_dpp(0, x, 0x118, 0xf, 0xf, false);
  return x;
}
__device__ __forceinline__ float xmax(float x) {
  auto s = __builtin_amdgcn_permlane16_swap(__float_as_uint(x), __float_as_uint(x), false, false);
  x = fmaxf(__uint_as_float(s[0]), __uint_as_float(s[1]));
  auto t = __builtin_amdgcn_permlane32_swap(__float_as_uint(x), __float_as_uint(x), false, false);
  return fmaxf(__uint_as_float(t[0]), __uint_as_float(t[1]));
}
__device__ __forceinline__ float xsum(float x) {
  auto s = __builtin_amdgcn_permlane16_swap(__float_as_uint(x), __float_as_uint(x), false, false);
  x = __uint_as_float(s[0]) + __uint_as_float(s[1]);
  auto t = __builtin_amdgcn_permlane32_swap(__float_as_uint(x), __float_as_uint(x), false, false);
  return __uint_as_float(t[0]) + __uint_as_float(t[1]);
}
__device__ __forceinline__ unsigned pk2(float a, float b) {
  f32x2 v = {a, b};
  return __builtin_bit_cast(unsigned, __builtin_convertvector(v, bf16x2));
}

#define KV_POL " nt"
__device__ __forceinline__ void dma4(unsigned ldsdst, unsigned voff, __amdgpu_buffer_rsrc_t srd, unsigned p0, unsigned p1, unsigned p2, unsigned p3) {
  asm volatile("s_mov_b32 m0, %0\n\ts_nop 3\n\tbuffer_load_dwordx4 %1, %2, %3 offen" KV_POL " lds\n\t"
               "s_add_u32 m0, m0, 0x1000\n\ts_nop 0\n\tbuffer_load_dwordx4 %1, %2, %4 offen" KV_POL " lds\n\t"
               "s_add_u32 m0, m0, 0x1000\n\ts_nop 0\n\tbuffer_load_dwordx4 %1, %2, %5 offen" KV_POL " lds\n\t"
               "s_add_u32 m0, m0, 0x1000\n\ts_nop 0\n\tbuffer_load_dwordx4 %1, %2, %6 offen" KV_POL " lds"
               :: "s"(ldsdst), "v"(voff), "s"(srd), "s"(p0), "s"(p1), "s"(p2), "s"(p3) : "memory", "scc");
}
__device__ __forceinline__ void dmaq(unsigned ldsdst, unsigned v0, unsigned v1, unsigned v2, unsigned v3, __amdgpu_buffer_rsrc_t srd, unsigned soff) {
  asm volatile("s_mov_b32 m0, %0\n\ts_nop 3\n\tbuffer_load_dwordx4 %1, %5, %6 offen lds\n\t"
               "s_add_u32 m0, m0, 0x400\n\ts_nop 0\n\tbuffer_load_dwordx4 %2, %5, %6 offen lds\n\t"
               "s_add_u32 m0, m0, 0x400\n\ts_nop 0\n\tbuffer_load_dwordx4 %3, %5, %6 offen lds\n\t"
               "s_add_u32 m0, m0, 0x400\n\ts_nop 0\n\tbuffer_load_dwordx4 %4, %5, %6 offen lds"
               :: "s"(ldsdst), "v"(v0), "v"(v1), "v"(v2), "v"(v3), "s"(srd), "s"(soff) : "memory", "scc");
}
template <int N> __device__ __forceinline__ void wait_bar() { asm volatile("s_waitcnt vmcnt(%0) lgkmcnt(0)\n\ts_barrier" :: "i"(N) : "memory"); }

struct Ctx {
  __amdgpu_buffer_rsrc_t srdK, srdV, srdQ, srdP;
  unsigned lds0, voffK, voffV, kaddr, vaddr;
  u32x4 pg;
  int info;
  int n, h, w, lane, me;
  unsigned pv0, pv1, pv2, pv3;
  int qready, curb;
  f32x4 o[8];
  bf16x8 qf[4], pk[2];
  float m, l;
};

__device__ __forceinline__ void issue_q(Ctx& c, int b) {
  const unsigned g = c.lane >> 4, ch = c.lane & 15;
  dmaq(c.lds0 + QOFF + c.w * 4096, g * 256 + ((ch ^ g) << 4), (4 + g) * 256 + ((ch ^ (4 + g)) << 4), (8 + g) * 256 + ((ch ^ (8 + g)) << 4),
       (12 + g) * 256 + ((ch ^ (12 + g)) << 4), c.srdQ, (unsigned)(b * HQ + 4 * c.h + c.w) * 4096u);
}
__device__ __forceinline__ void issue_group(Ctx& c, int g) {
  const unsigned dst = c.lds0 + (g & (NS - 1)) * SLOT + c.w * 1024;
  if (g >= 1 && g <= c.n) dma4(dst + VOFF, c.voffV, c.srdV, c.pv0, c.pv1, c.pv2, c.pv3);
  if (g < c.n) {
    c.pv0 = rl(c.pg[0], g); c.pv1 = rl(c.pg[1], g); c.pv2 = rl(c.pg[2], g); c.pv3 = rl(c.pg[3], g);
    dma4(dst, c.voffK, c.srdK, c.pv0, c.pv1, c.pv2, c.pv3);
  }
}
__device__ __forceinline__ void wait_group(int rem) {
  static_assert(NS == 4, "wait counts");
  if (rem >= 3) wait_bar<16>(); else if (rem == 2) wait_bar<12>(); else if (rem == 1) wait_bar<4>(); else wait_bar<0>();
}
__device__ __forceinline__ void publish(Ctx& c, int b, float m, float l) {
  const unsigned so = (unsigned)(c.h * 32 + c.me + b) * PO_SLOT + c.w * PO_WAVE, vo = c.lane * 16;
#pragma unroll
  for (int k = 0; k < 8; ++k) __builtin_amdgcn_raw_buffer_store_b128(__builtin_bit_cast(u32x4, c.o[k]), c.srdP, vo + k * 1024, so, 16);
  const u32x4 ml = {__float_as_uint(m), __float_as_uint(l), 0u, 0u};
  __builtin_amdgcn_raw_buffer_store_b128(ml, c.srdP, vo + 8 * 1024, so, 16);
}
__device__ __forceinline__ void read_v(const Ctx& c, int i, bf16x8 (&vf)[8][2]) {
  const unsigned base = c.vaddr + (i & (NS - 1)) * SLOT;
#pragma unroll
  for (int k = 0; k < 8; ++k) {
    const unsigned a = base ^ (k << 5);
#pragma unroll
    for (int u = 0; u < 2; ++u) {
      const bf16x4 lo = __builtin_amdgcn_ds_read_tr16_b64_v4bf16((LAS bf16x4*)(a + u * 8192));
      const bf16x4 hi = __builtin_amdgcn_ds_read_tr16_b64_v4bf16((LAS bf16x4*)(a + u * 8192 + 4096));
      vf[k][u] = __builtin_shufflevector(lo, hi, 0, 1, 2, 3, 4, 5, 6, 7);
    }
  }
}
__device__ __forceinline__ void pv(Ctx& c, const bf16x8 (&vf)[8][2]) {
#pragma unroll
  for (int u = 0; u < 2; ++u)
#pragma unroll
    for (int k = 0; k < 8; ++k) c.o[k] = __builtin_amdgcn_mfma_f32_16x16x32_bf16(vf[k][u], c.pk[u], c.o[k], 0, 0, 0);
}

template <bool PV> __device__ __forceinline__ void step(Ctx& c, int i) {
  wait_group(c.n - i);
  const int info = rl(c.info, i);
  const bool first = (info & I_FIRST) != 0;
  const int prevb = c.curb;
  if (first) {
    if (i < c.qready) asm volatile("s_waitcnt vmcnt(0)" ::: "memory");
    const unsigned qa = c.kaddr + QOFF + c.w * 4096;
#pragma unroll
    for (int s = 0; s < 4; ++s) c.qf[s] = *(LAS bf16x8*)(qa ^ (s << 6));
    asm volatile("s_waitcnt lgkmcnt(0)" : "+v"(c.qf[0]), "+v"(c.qf[1]), "+v"(c.qf[2]), "+v"(c.qf[3]) :: "memory");
    const int nb = info >> 20;
    if (nb) issue_q(c, nb - 1);
    c.qready = i + NS - 1;
    c.curb = (info >> 12) & 15;
  }
  bf16x8 kf[4][4];
  {
    const unsigned base = c.kaddr + (i & (NS - 1)) * SLOT;
#pragma unroll
    for (int s = 0; s < 4; ++s)
#pragma unroll
      for (int kb = 0; kb < 4; ++kb) kf[kb][s] = *(LAS bf16x8*)((base ^ (s << 6)) + kb * 4096);
  }
  issue_group(c, i + NS - 1);
  f32x4 s[4];
#pragma unroll
  for (int kb = 0; kb < 4; ++kb) s[kb] = f32x4{0.f, 0.f, 0.f, 0.f};
#pragma unroll
  for (int st = 0; st < 4; ++st)
#pragma unroll
    for (int kb = 0; kb < 4; ++kb) s[kb] = __builtin_amdgcn_mfma_f32_16x16x32_bf16(kf[kb][st], c.qf[st], s[kb], 0, 0, 0);
  bf16x8 vf[8][2];
  if (PV) read_v(c, i, vf);
  if ((info & (I_UNI | 64)) != 64) {
    const int nv = (info & 127) - 4 * (c.lane >> 4);
    const bool uni = (info & I_UNI) != 0;
#pragma unroll
    for (int kb = 0; kb < 4; ++kb)
#pragma unroll
      for (int r = 0; r < 4; ++r) s[kb][r] = (16 * kb + r < nv) ? (uni ? 0.f : s[kb][r]) : -__builtin_inff();
  }
  const float mold = c.m, lold = c.l;
  const float min_ = first ? -__builtin_inff() : mold, lin = first ? 0.f : lold;
  float t = fmaxf(fmaxf(s[0][0], s[0][1]), fmaxf(s[0][2], s[0][3]));
#pragma unroll
  for (int kb = 1; kb < 4; ++kb) t = fmaxf(fmaxf(fmaxf(t, s[kb][0]), fmaxf(s[kb][1], s[kb][2])), s[kb][3]);
  t = xmax(t);
  const float mn = fmaxf(min_, t * CSCALE);
  const float alpha = __builtin_amdgcn_exp2f(min_ - mn);
  float ps = 0.f;
#pragma unroll
  for (int kb = 0; kb < 4; ++kb)
#pragma unroll
    for (int r = 0; r < 4; ++r) { s[kb][r] = __builtin_amdgcn_exp2f(fmaf(s[kb][r], CSCALE, -mn)); ps += s[kb][r]; }
  if (PV) pv(c, vf);
#pragma unroll
  for (int u = 0; u < 2; ++u) {
    const u32x4 pw = {pk2(s[2 * u][0], s[2 * u][1]), pk2(s[2 * u][2], s[2 * u][3]), pk2(s[2 * u + 1][0], s[2 * u + 1][1]), pk2(s[2 * u + 1][2], s[2 * u + 1][3])};
    c.pk[u] = __builtin_bit_cast(bf16x8, pw);
  }
  c.m = mn; c.l = fmaf(lin, alpha, ps);
  if (PV) {
    if (first) {
      publish(c, prevb, mold, lold);
#pragma unroll
      for (int k = 0; k < 8; ++k) c.o[k] = f32x4{0.f, 0.f, 0.f, 0.f};
    } else if (__builtin_amdgcn_ballot_w64(mn > mold) != 0) {
#pragma unroll
      for (int k = 0; k < 8; ++k) c.o[k] *= alpha;
    }
  }
}

__global__ __launch_bounds__(512) void paged_attn_kernel(const char* __restrict__ q, const char* __restrict__ kc, const char* __restrict__ vc,
                                                            const int* __restrict__ bt, const int* __restrict__ lens, char* __restrict__ out,
                                                            char* ws, unsigned kvbytes, unsigned pad_) {
  __shared__ __attribute__((aligned(1024))) char lds[LDS_BYTES];
  TAKE(0);
  Ctx c;
  const int bid = blockIdx.x, lane = threadIdx.x & 63, w = __builtin_amdgcn_readfirstlane(threadIdx.x >> 6);
  const bool helper = bid >= 128;
  const int h = bid & 7, me = (bid >> 3) & 15;
  c.h = h; c.w = w; c.lane = lane; c.me = me;
  c.lds0 = (unsigned)(size_t)(LAS char*)lds;
  c.srdK = __builtin_amdgcn_make_buffer_rsrc((void*)kc, (short)0, (int)kvbytes, 0x00020000);
  c.srdV = __builtin_amdgcn_make_buffer_rsrc((void*)vc, (short)0, (int)kvbytes, 0x00020000);
  c.srdQ = __builtin_amdgcn_make_buffer_rsrc((void*)q, (short)0, NB * HQ * SQ * HD * 2, 0x00020000);
  c.srdP = __builtin_amdgcn_make_buffer_rsrc((void*)(ws + PO_OFF), (short)0, (int)PO_BYTES, 0x00020000);
  const unsigned g = lane >> 4, ch = lane & 15;
  {
    const unsigned row = 4 * w + g;
    c.voffK = row * 2048 + h * 256 + ((ch ^ row) << 4);
    c.voffV = row * 2048 + h * 256 + ((ch ^ (2 * (row & 7))) << 4);
    c.kaddr = c.lds0 + ch * 256 + ((g ^ ch) << 4);
    const unsigned vr = 4 * g + (ch >> 2), p = lane & 3;
    c.vaddr = c.lds0 + VOFF + vr * 256 + (((p >> 1) ^ (2 * (vr & 7))) << 4) + 8 * (p & 1);
  }
  int len = lens[lane & 15];
  u32x4 own = {0u, 0u, 0u, 0u};
  if (!helper) { own = *(const u32x4*)(bt + me * NBPS + 4 * lane); issue_q(c, me); }
  const bool uni = len <= 0;
  len = uni ? SKV : min(len, SKV);
  const int P = (len + 15) >> 4;
  const int T = rl(scan16(P), 15);
  const int R = (T + 16 * XSLACK + 31) >> 5;
  const int F = min(P, R), L = P - F;
  const int Ci = scan16(L), C = Ci - L, H = rl(Ci, 15);
  unsigned mask;
  if (!helper) {
    const int Fb = rl(F, me), lenb = rl(len, me), Cb = rl(C, me), Lb = rl(L, me), ub = rl(uni, me);
    c.n = (Fb + 3) >> 2;
    const int nv = max(0, min(64, min(lenb, 16 * Fb) - 64 * lane));
    c.pg[0] = own[0] << 15;
    c.pg[1] = (4 * lane + 1 < Fb ? own[1] : own[0]) << 15;
    c.pg[2] = (4 * lane + 2 < Fb ? own[2] : own[0]) << 15;
    c.pg[3] = (4 * lane + 3 < Fb ? own[3] : own[0]) << 15;
    c.info = nv | (lane == 0 ? I_FIRST : 0) | (ub ? I_UNI : 0) | (me << 12);
    const int j = lane & 15, h0 = (j * H) >> 4, h1 = ((j + 1) * H) >> 4;
    mask = (unsigned)__builtin_amdgcn_ballot_w64(max(h0, Cb) < min(h1, Cb + Lb)) & 0xffffu;
  } else {
    const int h0 = (me * H) >> 4, h1 = ((me + 1) * H) >> 4;
    const int s0 = max(h0 - C, 0), cnt = max(min(h1 - C, L) - s0, 0), p0 = F + s0, nt = (cnt + 3) >> 2;
    const int TPi = scan16(nt), TP = TPi - nt;
    c.n = rl(TPi, 15);
    mask = (unsigned)__builtin_amdgcn_ballot_w64(cnt > 0) & 0xffffu;
    if (mask == 0) return;
    issue_q(c, __builtin_ctz(mask));
    int i0 = 0, i1 = 0, inf = 0;
    for (unsigned mm = mask; mm;) {
      const int b = __builtin_ctz(mm);
      mm &= mm - 1;
      const int tp = rl(TP, b), p0b = rl(p0, b), cntb = rl(cnt, b), lenb = rl(len, b), ub = rl(uni, b);
      const int k = lane - tp;
      if (k >= 0 && 4 * k < cntb) {
        i0 = b * NBPS + p0b + 4 * k; i1 = b * NBPS + p0b + cntb - 1;
        const int nv = max(0, min(64, min(lenb, 16 * (p0b + cntb)) - 16 * (p0b + 4 * k)));
        inf = nv | (k == 0 ? I_FIRST : 0) | (ub ? I_UNI : 0) | (b << 12) | ((mm ? __builtin_ctz(mm) + 1 : 0) << 20);
      }
    }
    c.info = inf;
    c.pg[0] = (unsigned)bt[i0] << 15; c.pg[1] = (unsigned)bt[min(i0 + 1, i1)] << 15;
    c.pg[2] = (unsigned)bt[min(i0 + 2, i1)] << 15; c.pg[3] = (unsigned)bt[min(i0 + 3, i1)] << 15;
  }
  asm volatile("" : "+v"(c.pg), "+v"(c.info));
  TAKE(1);
  c.pv0 = c.pv1 = c.pv2 = c.pv3 = 0;
  c.qready = 0; c.curb = me;
  c.m = -__builtin_inff(); c.l = 0.f;
#pragma unroll
  for (int k = 0; k < 8; ++k) c.o[k] = f32x4{0.f, 0.f, 0.f, 0.f};
#pragma unroll
  for (int gg = 0; gg < NS - 1; ++gg) issue_group(c, gg);
  step<false>(c, 0);
  TAKE(2);
  for (int i = 1; i < c.n; ++i) step<true>(c, i);
  TAKE(3);
  GAS char* rec = (GAS char*)ws + (unsigned)(h * 32 + me + (lane & 15)) * REC_STRIDE;
  const bool mine = (mask >> (lane & 15)) & 1;
  if (helper) {
    wait_bar<0>();
    TAKE(4);
    bf16x8 vf[8][2];
    read_v(c, c.n, vf);
    const u64 base = __hip_atomic_load((GAS u64*)rec, __ATOMIC_RELAXED, __HIP_MEMORY_SCOPE_AGENT);
    pv(c, vf);
    publish(c, c.curb, c.m, c.l);
    wait_bar<0>();
    if (w == 0 && lane < 16 && mine) __hip_atomic_store((GAS u64*)(rec + 8), base + TAG_STEP, __ATOMIC_RELAXED, __HIP_MEMORY_SCOPE_AGENT);
    TAKE(7); FLUSH_STAMPS();
    return;
  }
  wait_bar<0>();
  TAKE(4);
  {
    bf16x8 vf[8][2];
    read_v(c, c.n, vf);
    pv(c, vf);
  }
  u64 tag = 0;
  if (mask) {
    tag = __hip_atomic_load((GAS u64*)rec, __ATOMIC_RELAXED, __HIP_MEMORY_SCOPE_AGENT) + TAG_STEP;
    for (unsigned spins = 0; spins < (1u << 18); ++spins) {
      const u64 f = __hip_atomic_load((GAS u64*)(rec + 8), __ATOMIC_RELAXED, __HIP_MEMORY_SCOPE_AGENT);
      if (__builtin_amdgcn_ballot_w64(mine && f != tag) == 0) break;
      __builtin_amdgcn_s_sleep(2);
    }
    __builtin_amdgcn_fence(__ATOMIC_ACQUIRE, "wavefront");
    TAKE(5);
    for (unsigned mm = mask; mm;) {
      u32x4 pr[3][9];
      bool ok[3];
#pragma unroll
      for (int a = 0; a < 3; ++a) {
        ok[a] = mm != 0;
        const unsigned so = ok[a] ? (unsigned)(h * 32 + me + __builtin_ctz(mm)) * PO_SLOT + w * PO_WAVE : PO_BYTES;
        mm &= mm - 1;
#pragma unroll
        for (int k = 0; k < 9; ++k) pr[a][k] = __builtin_amdgcn_raw_buffer_load_b128(c.srdP, lane * 16 + k * 1024, so, 16);
      }
      float mj[3], mn = c.m;
#pragma unroll
      for (int a = 0; a < 3; ++a) { mj[a] = ok[a] ? __uint_as_float(pr[a][8][0]) : -__builtin_inff(); mn = fmaxf(mn, mj[a]); }
      const float a0 = __builtin_amdgcn_exp2f(c.m - mn);
      c.l *= a0;
#pragma unroll
      for (int k = 0; k < 8; ++k) c.o[k] *= a0;
#pragma unroll
      for (int a = 0; a < 3; ++a) {
        const float wj = __builtin_amdgcn_exp2f(mj[a] - mn);
        c.l = fmaf(wj, __uint_as_float(pr[a][8][1]), c.l);
#pragma unroll
        for (int k = 0; k < 8; ++k) c.o[k] += wj * __builtin_bit_cast(f32x4, pr[a][k]);
      }
      c.m = mn;
    }
  }
  TAKE(6);
  const float inv = 1.f / xsum(c.l);
  char* orow = out + ((size_t)((me * HQ + 4 * h + w) * SQ + ch) * HD + 16 * (g & 1) + 8 * (g >> 1)) * 2;
#pragma unroll
  for (int k = 0; k < 8; k += 2) {
    const unsigned a0 = pk2(c.o[k][0] * inv, c.o[k][1] * inv), a1 = pk2(c.o[k][2] * inv, c.o[k][3] * inv);
    const unsigned b0 = pk2(c.o[k + 1][0] * inv, c.o[k + 1][1] * inv), b1 = pk2(c.o[k + 1][2] * inv, c.o[k + 1][3] * inv);
    auto r0 = __builtin_amdgcn_permlane16_swap(a0, b0, false, false);
    auto r1 = __builtin_amdgcn_permlane16_swap(a1, b1, false, false);
    *(u32x4*)(orow + k * 32) = u32x4{r0[0], r1[0], r0[1], r1[1]};
  }
  if (mask) {
    asm volatile("s_barrier" ::: "memory");
    if (w == 0 && lane < 16 && mine) __hip_atomic_store((GAS u64*)rec, tag, __ATOMIC_RELAXED, __HIP_MEMORY_SCOPE_AGENT);
  }
  TAKE(7); FLUSH_STAMPS();
}

}

extern "C" void kernel_launch(void* const* d_in, const int* in_sizes, int n_in, void* d_out, int out_size, void* d_ws, size_t ws_size,
                              hipStream_t stream) {
  (void)n_in; (void)out_size; (void)ws_size;
  hipLaunchKernelGGL(paged_attn_kernel, dim3(256), dim3(256), 0, stream, (const char*)d_in[0], (const char*)d_in[1], (const char*)d_in[2],
                     (const int*)d_in[3], (const int*)d_in[4], (char*)d_out, (char*)d_ws, (unsigned)in_sizes[1] * 2u, 0u);
}
```

```cpp
#include <hip/hip_runtime.h>
#include <stdint.h>

#define XP 8
#define LOADER0 0
#define WCOMP 1
#define PBF16 1
#define XMAP 0
#define W0 24
#define WEND 36
#define OUT_AUX -1
#define WLEAD 0

#define TAKE(k) do {} while (0)
#define FLUSH_STAMPS() do {} while (0)

namespace {

typedef __attribute__((ext_vector_type(8))) __bf16 bf16x8;
typedef __attribute__((ext_vector_type(4))) __bf16 bf16x4;
typedef __attribute__((ext_vector_type(2))) __bf16 bf16x2;
typedef __attribute__((ext_vector_type(4))) float f32x4;
typedef __attribute__((ext_vector_type(2))) float f32x2;
typedef __attribute__((ext_vector_type(4))) unsigned u32x4;
typedef __attribute__((ext_vector_type(2))) unsigned u32x2;
typedef unsigned long long u64;
#define LAS __attribute__((address_space(3)))
#define GAS __attribute__((address_space(1)))

constexpr int HQ = 32, SQ = 16, HD = 128, NBPS = 256, SKV = 4096;
constexpr int NS = 4;
constexpr unsigned SLOT = 32768, VOFF = 16384, DONE_OFF = NS * SLOT, FULL_OFF = DONE_OFF + 16, LDS_BYTES = FULL_OFF + 16;
constexpr unsigned REC_STRIDE = 128;
constexpr unsigned PO_OFF = 65536, PO_WAVE = 9 * 1024, PO_SLOT = 4 * PO_WAVE, PO_BYTES = 256 * PO_SLOT;
constexpr u64 TAG_STEP = 0x9E3779B97F4A7C15ull;
constexpr float CSCALE = 0.08838834764831845f * 1.4426950408889634f;
static_assert(W0 >= 8 && W0 <= 60 && WEND >= 8 && WEND <= 60, "window");
#define THR 12.f
constexpr unsigned SPIN_CAP = 1u << 20;

#define MX(a, b) __builtin_elementwise_maximum((a), (b))
__device__ __forceinline__ int rl(int v, int l) { return __builtin_amdgcn_readlane(v, l); }
__device__ __forceinline__ int scan16(int x) {
  x += __builtin_amdgcn_update_dpp(0, x, 0x111, 0xf, 0xf, false);
  x += __builtin_amdgcn_update_dpp(0, x, 0x112, 0xf, 0xf, false);
  x += __builtin_amdgcn_update_dpp(0, x, 0x114, 0xf, 0xf, false);
  x += __builtin_amdgcn_update_dpp(0, x, 0x118, 0xf, 0xf, false);
  return x;
}
__device__ __forceinline__ float xmax(float x) {
  auto s = __builtin_amdgcn_permlane16_swap(__float_as_uint(x), __float_as_uint(x), false, false);
  x = MX(__uint_as_float(s[0]), __uint_as_float(s[1]));
  auto t = __builtin_amdgcn_permlane32_swap(__float_as_uint(x), __float_as_uint(x), false, false);
  return MX(__uint_as_float(t[0]), __uint_as_float(t[1]));
}
__device__ __forceinline__ unsigned pk2(float a, float b) {
  f32x2 v = {a, b};
  return __builtin_bit_cast(unsigned, __builtin_convertvector(v, bf16x2));
}

#define K_POL " nt"
#define V_POL " nt"
#define DMA_PAGE(POL) \
  asm volatile("s_mov_b32 m0, %0\n\ts_nop 3\n\tbuffer_load_dwordx4 %1, %5, %6 offen" POL " lds\n\t" \
               "buffer_load_dwordx4 %2, %5, %6 offen offset:1024" POL " lds\n\t" \
               "buffer_load_dwordx4 %3, %5, %6 offen offset:2048" POL " lds\n\t" \
               "buffer_load_dwordx4 %4, %5, %6 offen offset:3072" POL " lds" \
               :: "s"(ldsdst), "v"(voff[0]), "v"(voff[1]), "v"(voff[2]), "v"(voff[3]), "s"(srd), "s"(soff) : "memory")
__device__ __forceinline__ void dma_vk(unsigned ldsdst, u32x4 voff, __amdgpu_buffer_rsrc_t srd, unsigned soff) { DMA_PAGE(K_POL); }
__device__ __forceinline__ void dma_vv(unsigned ldsdst, u32x4 voff, __amdgpu_buffer_rsrc_t srd, unsigned soff) { DMA_PAGE(V_POL); }
#define dma_page(dst, vo, srd, p) dma_##vo(dst, vo, srd, p)
#define WT1(k) "s_waitcnt vmcnt(" #k ")\n\ts_setpc_b64 s[60:61]\n\t"
#define WT8(a, b, c, d, e, f, g, h) WT1(a) WT1(b) WT1(c) WT1(d) WT1(e) WT1(f) WT1(g) WT1(h)
__device__ __forceinline__ u64 wait_tab() {
  u64 a;
  asm volatile("s_getpc_b64 %0\n\ts_branch .Lwt_end%=\n\t"
               WT8(0, 1, 2, 3, 4, 5, 6, 7) WT8(8, 9, 10, 11, 12, 13, 14, 15) WT8(16, 17, 18, 19, 20, 21, 22, 23) WT8(24, 25, 26, 27, 28, 29, 30, 31)
               WT8(32, 33, 34, 35, 36, 37, 38, 39) WT8(40, 41, 42, 43, 44, 45, 46, 47) WT8(48, 49, 50, 51, 52, 53, 54, 55) WT8(56, 57, 58, 59, 60, 61, 62, 63)
               ".Lwt_end%=:" : "=s"(a));
  return a + 4;
}
__device__ __forceinline__ void wait_dyn(u64 entry) { asm volatile("s_swappc_b64 s[60:61], %0" :: "s"(entry) : "memory", "s60", "s61"); }
__device__ __forceinline__ void bar() { asm volatile("s_barrier" ::: "memory"); }
template <int N> __device__ __forceinline__ void wait_vm() { asm volatile("s_waitcnt vmcnt(%0)" :: "i"(N) : "memory"); }
__device__ __forceinline__ void lds_bar() { asm volatile("s_waitcnt lgkmcnt(0)\n\ts_barrier" ::: "memory"); }

constexpr int NPR = PBF16 ? 4 : 8;
__device__ __forceinline__ void lds_put(unsigned addr, int v) { asm volatile("ds_write_b32 %0, %1" :: "v"(addr), "v"(v) : "memory"); }
__device__ __forceinline__ int lds_get(unsigned addr) {
  int v;
  asm volatile("ds_read_b32 %0, %1\n\ts_waitcnt lgkmcnt(0)" : "=v"(v) : "v"(addr) : "memory");
  return __builtin_amdgcn_readfirstlane(v);
}
__device__ __forceinline__ int lds_min4(unsigned addr) {
  u32x4 v;
  asm volatile("ds_read_b128 %0, %1\n\ts_waitcnt lgkmcnt(0)" : "=v"(v) : "v"(addr) : "memory");
  return __builtin_amdgcn_readfirstlane(min(min(v[0], v[1]), min(v[2], v[3])));
}
struct Part { u32x4 o[NPR]; u32x2 ml; };
struct Ctx {
  unsigned kaddr, vaddr, fulla, donea;
  int lane, full;
  f32x4 o[8];
  bf16x8 qf[4], qn[4], pk[2], ones;
  f32x4 l;
  float m;
  Part held;
};

__device__ __forceinline__ void pack_part(Part& p, const f32x4 (&o)[8], float m, float l) {
#pragma unroll
  for (int k = 0; k < NPR; ++k) {
    if (PBF16) p.o[k] = u32x4{pk2(o[2 * k][0], o[2 * k][1]), pk2(o[2 * k][2], o[2 * k][3]), pk2(o[2 * k + 1][0], o[2 * k + 1][1]), pk2(o[2 * k + 1][2], o[2 * k + 1][3])};
    else p.o[k] = __builtin_bit_cast(u32x4, o[k]);
  }
  p.ml = u32x2{__float_as_uint(m), __float_as_uint(l)};
}
__device__ __forceinline__ f32x4 part_o(const Part& p, int k) {
  if (PBF16) {
    const unsigned a = p.o[k >> 1][2 * (k & 1)], b = p.o[k >> 1][2 * (k & 1) + 1];
    return f32x4{__uint_as_float(a << 16), __uint_as_float(a & 0xffff0000u), __uint_as_float(b << 16), __uint_as_float(b & 0xffff0000u)};
  }
  return __builtin_bit_cast(f32x4, p.o[k & (NPR - 1)]);
}
__device__ __forceinline__ void store_part(const Part& p, __amdgpu_buffer_rsrc_t srd, int lane, unsigned so) {
#pragma unroll
  for (int k = 0; k < NPR; ++k) __builtin_amdgcn_raw_buffer_store_b128(p.o[k], srd, lane * 16 + (k & 3) * 1024, so + (k >> 2) * 4096, 16);
  __builtin_amdgcn_raw_buffer_store_b64(p.ml, srd, lane * 8, so + NPR * 1024, 16);
}
__device__ __forceinline__ void load_part(Part& p, __amdgpu_buffer_rsrc_t srd, int lane, unsigned so) {
  p.ml = __builtin_amdgcn_raw_buffer_load_b64(srd, lane * 8, so + NPR * 1024, 16);
#pragma unroll
  for (int k = 0; k < NPR; ++k) p.o[k] = __builtin_amdgcn_raw_buffer_load_b128(srd, lane * 16 + (k & 3) * 1024, so + (k >> 2) * 4096, 16);
}
__device__ __forceinline__ void read_v(const Ctx& c, int t, bf16x8 (&vf)[8][2]) {
  const unsigned base = c.vaddr + (t & (NS - 1)) * SLOT;
#pragma unroll
  for (int k = 0; k < 8; ++k) {
    const unsigned a = base ^ (k << 5);
#pragma unroll
    for (int u = 0; u < 2; ++u) {
      const bf16x4 lo = __builtin_amdgcn_ds_read_tr16_b64_v4bf16((LAS bf16x4*)(a + u * 8192));
      const bf16x4 hi = __builtin_amdgcn_ds_read_tr16_b64_v4bf16((LAS bf16x4*)(a + u * 8192 + 4096));
      vf[k][u] = __builtin_shufflevector(lo, hi, 0, 1, 2, 3, 4, 5, 6, 7);
    }
  }
}
__device__ __forceinline__ void pv(Ctx& c, const bf16x8 (&vf)[8][2]) {
#pragma unroll
  for (int u = 0; u < 2; ++u)
#pragma unroll
    for (int k = 0; k < 8; ++k) c.o[k] = __builtin_amdgcn_mfma_f32_16x16x32_bf16(vf[k][u], c.pk[u], c.o[k], 0, 0, 0);
  c.l = __builtin_amdgcn_mfma_f32_16x16x32_bf16(c.ones, c.pk[0], c.l, 0, 0, 0);
  c.l = __builtin_amdgcn_mfma_f32_16x16x32_bf16(c.ones, c.pk[1], c.l, 0, 0, 0);
}
__device__ __forceinline__ void acquire(Ctx& c, int i) {
  for (unsigned spins = 0; c.full <= i && spins < SPIN_CAP; ++spins) {
    c.full = lds_get(c.fulla);
    if (c.full <= i) __builtin_amdgcn_s_sleep(1);
  }
  asm volatile("" ::: "memory");
}
__device__ __forceinline__ void retire(Ctx& c, int i) {
  asm volatile("s_waitcnt lgkmcnt(0)\n\tds_write_b32 %0, %1" :: "v"(c.donea), "v"(i + 1) : "memory");
}
template <bool PV, bool OPEN> __device__ __forceinline__ void step(Ctx& c, int i, int nv) {
  if (PV && OPEN) {
#pragma unroll
    for (int s = 0; s < 4; ++s) c.qf[s] = c.qn[s];
  }
  bf16x8 kf[4][4];
  {
    const unsigned base = c.kaddr + (i & (NS - 1)) * SLOT;
#pragma unroll
    for (int s = 0; s < 4; ++s)
#pragma unroll
      for (int kb = 0; kb < 4; ++kb) kf[kb][s] = *(LAS bf16x8*)((base ^ (s << 6)) + kb * 4096);
  }
  __builtin_amdgcn_sched_barrier(0);
  f32x4 s[4];
#pragma unroll
  for (int kb = 0; kb < 4; ++kb) s[kb] = f32x4{0.f, 0.f, 0.f, 0.f};
#pragma unroll
  for (int st = 0; st < 4; ++st)
#pragma unroll
    for (int kb = 0; kb < 4; ++kb) s[kb] = __builtin_amdgcn_mfma_f32_16x16x32_bf16(kf[kb][st], c.qf[st], s[kb], 0, 0, 0);
  bf16x8 vf[8][2];
  if (PV) {
    read_v(c, i - 1, vf);
#pragma unroll
    for (int k = 0; k < 16; ++k) { __builtin_amdgcn_sched_group_barrier(0x8, 1, 0); __builtin_amdgcn_sched_group_barrier(0x2, 1, 0); __builtin_amdgcn_sched_group_barrier(0x100, 2, 0); }
    __builtin_amdgcn_sched_barrier(0);
  }
  if (!PV && nv < 64) {
    const int lim = nv - 4 * (c.lane >> 4);
#pragma unroll
    for (int kb = 0; kb < 4; ++kb)
#pragma unroll
      for (int r = 0; r < 4; ++r) s[kb][r] = (16 * kb + r < lim) ? s[kb][r] : -__builtin_inff();
  }
  const float t0 = MX(MX(MX(MX(s[0][0], s[0][1]), s[0][2]), s[0][3]), s[1][0]), t1 = MX(MX(MX(MX(s[1][1], s[1][2]), s[1][3]), s[2][0]), s[2][1]);
  const float t2 = MX(MX(MX(MX(s[2][2], s[2][3]), s[3][0]), s[3][1]), s[3][2]);
  const float t = MX(MX(MX(t0, t1), t2), s[3][3]);
  const float mold = c.m;
  const bool bump = OPEN || __builtin_expect(__builtin_amdgcn_ballot_w64(!(fmaf(t, CSCALE, -mold) <= THR)) != 0, 0);
  if (bump) {
    const float tm = xmax(t) * CSCALE;
    c.m = OPEN ? tm : MX(mold, tm);
  }
#pragma unroll
  for (int kb = 0; kb < 4; ++kb)
#pragma unroll
    for (int r = 0; r < 4; ++r) s[kb][r] = __builtin_amdgcn_exp2f(fmaf(s[kb][r], CSCALE, -c.m));
  if (PV) pv(c, vf);
#pragma unroll
  for (int u = 0; u < 2; ++u) {
    const u32x4 pw = {pk2(s[2 * u][0], s[2 * u][1]), pk2(s[2 * u][2], s[2 * u][3]), pk2(s[2 * u + 1][0], s[2 * u + 1][1]), pk2(s[2 * u + 1][2], s[2 * u + 1][3])};
    c.pk[u] = __builtin_bit_cast(bf16x8, pw);
  }
  if (PV) {
#pragma unroll
    for (int k = 0; k < 16; ++k) { __builtin_amdgcn_sched_group_barrier(0x8, 1, 1); __builtin_amdgcn_sched_group_barrier(0x402, 2, 1); }
    asm volatile("" : "+v"(c.pk[0]), "+v"(c.pk[1]));
    if (OPEN) {
      pack_part(c.held, c.o, mold, c.l[0]);
#pragma unroll
      for (int k = 0; k < 8; ++k) c.o[k] = f32x4{0.f, 0.f, 0.f, 0.f};
      c.l = f32x4{0.f, 0.f, 0.f, 0.f};
    } else if (bump) {
      const float alpha = __builtin_amdgcn_exp2f(mold - c.m);
#pragma unroll
      for (int k = 0; k < 8; ++k) c.o[k] *= alpha;
      c.l *= alpha;
    }
  }
}
template <int R> __device__ __forceinline__ int rank16(int key) {
  if constexpr (R == 0) return 0;
  else return rank16<R - 1>(key) + (__builtin_amdgcn_update_dpp(0, key, 0x120 + R, 0xf, 0xf, false) > key);
}
struct Split { int n0, n, b2, t2, dep2, share; };
__device__ __forceinline__ Split split_work(int len, int me, bool helper) {
  const int P = (len + 15) >> 4, rank = rank16<15>(P * 16 + 15 - (int)(threadIdx.x & 15));
  const int tot = rl(scan16(P), 15);
  const int rk = rl(rank, me), other = __builtin_ctz((unsigned)__builtin_amdgcn_ballot_w64(rank == 15 - rk));
  const bool isL = rk < 8;
  const int Pm = rl(P, me), Po = rl(P, other), L = isL ? Pm : Po, S = isL ? Po : Pm, tL = (L + 3) >> 2, tS = (S + 3) >> 2;
  const int a2 = (L + S + 2 * XP + 4) >> 3;
  const int aS = min(a2 >> 1, tS - 1);
  int aL = a2 - (a2 >> 1);
  if (aS < (a2 >> 1)) aL = (int)(((unsigned)(L + S - 4 * aS + 2 * XP + 6) * 21846u) >> 18);
  aL = min(aL, tL - 1);
  const int R = L + S - 4 * (aL + aS);
  const int kL = max(aL, min(tL - 1, (2 * L - R + 4) >> 3));
  Split r;
  r.b2 = other; r.t2 = kL; r.dep2 = isL && kL > aL;
  int pages;
  if (!helper) { r.n0 = r.n = isL ? aL : aS; pages = 4 * r.n - XP; }
  else if (isL) { r.n0 = r.n = tL - kL; pages = L - 4 * kL; }
  else { r.n0 = tS - aS; r.n = r.n0 + kL - aL; pages = S - 4 * aS + 4 * (kL - aL); }
  r.share = WCOMP ? __builtin_amdgcn_readfirstlane((int)((float)(pages * 32 * 256) * __builtin_amdgcn_rcpf((float)max(tot - 16 * XP, 16)))) : 256;
  return r;
}
__device__ __forceinline__ void load_q(bf16x8 (&qf)[4], const char* qrow, int b) {
#pragma unroll
  for (int s = 0; s < 4; ++s) qf[s] = *(const bf16x8*)(qrow + (size_t)b * (HQ * SQ * HD * 2) + s * 64);
}
__device__ __forceinline__ void zero_q(bf16x8 (&qf)[4]) {
#pragma unroll
  for (int s = 0; s < 4; ++s) qf[s] = __builtin_bit_cast(bf16x8, u32x4{0u, 0u, 0u, 0u});
}

__global__ __launch_bounds__(320) void paged_attn_kernel(const char* __restrict__ q, const char* __restrict__ kc, const char* __restrict__ vc,
                                                        const int* __restrict__ bt, const int* __restrict__ lens, char* __restrict__ out,
                                                        char* ws, unsigned kvbytes, unsigned pad_) {
  __shared__ __attribute__((aligned(1024))) char lds[LDS_BYTES];
  TAKE(0);
  const int bid = blockIdx.x, lane = threadIdx.x & 63, wv = __builtin_amdgcn_readfirstlane(threadIdx.x >> 6), w = LOADER0 ? (wv ? wv - 1 : 4) : wv;
  const bool helper = bid >= 128, loader = w == 4;
  const int h = bid & 7, me = (bid >> 3) & 15;
  const unsigned lds0 = (unsigned)(size_t)(LAS char*)lds;
  const unsigned g = lane >> 4, ch = lane & 15;
  lds_put(lds0 + DONE_OFF + 4 * w, 0);

  if (__builtin_expect(loader, 1)) {
    __builtin_amdgcn_s_setprio(3);
    u32x4 pg = *(const u32x4*)(bt + me * NBPS + 4 * lane);
    int len = lens[ch];
    const __amdgpu_buffer_rsrc_t srdK = __builtin_amdgcn_make_buffer_rsrc((void*)kc, (short)0, (int)kvbytes, 0x00020000);
    const __amdgpu_buffer_rsrc_t srdV = __builtin_amdgcn_make_buffer_rsrc((void*)vc, (short)0, (int)kvbytes, 0x00020000);
    u32x4 vk, vv;
#pragma unroll
    for (int i = 0; i < 4; ++i) {
      const unsigned row = 4 * i + g;
      vk[i] = row * 2048 + h * 256 + ((ch ^ row) << 4) - i * 1024;
      vv[i] = row * 2048 + h * 256 + ((ch ^ (2 * (row & 7))) << 4) - i * 1024;
    }
    asm volatile("" : "+v"(vk), "+v"(vv) : "s"(srdK), "s"(srdV), "s"(ws));
    __builtin_amdgcn_sched_barrier(0);
    len = len <= 0 ? SKV : min(len, SKV);
    pg <<= 15;
    const int Pm = (rl(len, me) + 15) >> 4, tf = helper ? (Pm - 1) >> 2 : 0, cut0 = helper ? 4 * (Pm - 4 * tf) : 16;
    unsigned p0 = rl(pg[0], tf), p1 = rl(pg[1], tf), p2 = rl(pg[2], tf), p3 = rl(pg[3], tf);
    dma_page(lds0, vk, srdK, p0);
    if (cut0 > 4) dma_page(lds0 + 4096, vk, srdK, p1);
    if (cut0 > 8) dma_page(lds0 + 8192, vk, srdK, p2);
    if (cut0 > 12) dma_page(lds0 + 12288, vk, srdK, p3);
    dma_page(lds0 + VOFF, vv, srdV, p0);
    if (cut0 > 4) dma_page(lds0 + VOFF + 4096, vv, srdV, p1);
    __builtin_amdgcn_sched_barrier(0);
    for (int k = cut0; k < 16; ++k) *(LAS u32x4*)(lds0 + VOFF + k * 1024 + lane * 16) = u32x4{0u, 0u, 0u, 0u};
    lds_bar();
    const Split sp = split_work(len, me, helper);
    const int n0 = sp.n0, n = sp.n;
    u32x4 pg2;
    asm volatile("global_load_dwordx4 %0, %1, off" : "=v"(pg2) : "v"(bt + sp.b2 * NBPS + 4 * lane) : "memory");
    GAS char* rec = (GAS char*)ws + (unsigned)(((lane & 1 && helper ? sp.b2 : me) * 8 + h) * 2 + (lane & 1)) * REC_STRIDE;
    const bool mine = !(lane & 1) || (helper ? n > n0 : sp.dep2);
    const unsigned fulla = lds0 + FULL_OFF, donea = lds0 + DONE_OFF;
    const int tend = helper ? n : n - (XP + 3) / 4 - WLEAD;
    const int wfix = min(max(2 * 256 + (W0 - 2) * sp.share, 8 * 256), 59 * 256);
    const u64 tab = wait_tab();
    const int mark = cut0 + min(cut0, 8);
    int win = wfix >> 8, acc = 128, issued = mark, rel = 0, kend = cut0, freed = 0;
    u64 ent = tab + 8 * (win - 4);
#define RELSTAMP()
    auto release = [&]() { RELSTAMP() ++rel; kend = rel >= n ? 1 << 20 : 32 * rel - 16 + 2 * cut0; lds_put(fulla, rel); };
    auto need = [&](int k) { for (unsigned spins = 0; freed < k && spins < SPIN_CAP; ++spins) freed = lds_min4(donea); };
#define CHUNK(dst, vo, srd, p) { wait_dyn(ent); dma_page(dst, vo, srd, p); if (issued - win + 4 >= kend) release(); issued += 4; }
    if (n > 0) {
      if (cut0 > 8) CHUNK(lds0 + VOFF + 8192, vv, srdV, p2)
      if (cut0 > 12) CHUNK(lds0 + VOFF + 12288, vv, srdV, p3)
    }
    int tl = tf;
    for (int t = 1; t < n; ++t) {
      acc = (acc & 255) + wfix;
      win = t >= tend ? WEND : acc >> 8;
      ent = tab + 8 * (win - 4);
      tl += helper ? -1 : 1;
      if (t == n0) {
        wait_dyn(tab + 8 * min(issued - mark, 63));
        asm volatile("" : "+v"(pg2));
        pg = pg2 << 15;
        tl = sp.t2 - 1;
      }
      const unsigned dst = lds0 + (t & (NS - 1)) * SLOT;
      p0 = rl(pg[0], tl); p1 = rl(pg[1], tl); p2 = rl(pg[2], tl); p3 = rl(pg[3], tl);
      need(t - NS + 1);
      CHUNK(dst, vk, srdK, p0) CHUNK(dst + 4096, vk, srdK, p1) CHUNK(dst + 8192, vk, srdK, p2) CHUNK(dst + 12288, vk, srdK, p3)
      need(t - NS + 2);
      CHUNK(dst + VOFF, vv, srdV, p0) CHUNK(dst + VOFF + 4096, vv, srdV, p1) CHUNK(dst + VOFF + 8192, vv, srdV, p2) CHUNK(dst + VOFF + 12288, vv, srdV, p3)
    }
#undef CHUNK
    while (rel < n) { wait_dyn(tab + 8 * min(issued - kend, 63)); release(); }
    u32x4 rv;
    asm volatile("global_load_dwordx4 %0, %1, off sc1\n\ts_waitcnt vmcnt(1)\n\tds_write_b32 %2, %3\n\ts_waitcnt vmcnt(0)"
                 : "=&v"(rv) : "v"(rec), "v"(fulla), "v"(n + 1), "v"(pg2) : "memory");
    const u64 tag = (((u64)rv[1] << 32) | rv[0]) + TAG_STEP;
    if (!helper) {
      u64 f = ((u64)rv[3] << 32) | rv[2];
      for (unsigned spins = 0; spins < SPIN_CAP && __builtin_amdgcn_ballot_w64(mine && f != tag) != 0; ++spins)
        f = __hip_atomic_load((GAS u64*)(rec + 8), __ATOMIC_RELAXED, __HIP_MEMORY_SCOPE_AGENT);
      lds_put(fulla, n + 2);
    } else {
      bar();
    }
    if (lane < 2 && mine) __hip_atomic_store((GAS u64*)(rec + (helper ? 8 : 0)), tag, __ATOMIC_RELAXED, __HIP_MEMORY_SCOPE_AGENT);
    return;
  }

  const char* qrow = q + ((4 * h + w) * SQ + ch) * (HD * 2) + g * 16;
  Ctx c;
  int len = lens[ch];
  load_q(c.qf, qrow, me);
  __builtin_amdgcn_sched_barrier(0);
  lds_bar();
  __builtin_amdgcn_sched_barrier(0);
  const unsigned unim = (unsigned)__builtin_amdgcn_ballot_w64(len <= 0);
  len = len <= 0 ? SKV : min(len, SKV);
  const Split sp = split_work(len, me, helper);
  const int n0 = sp.n0, n = sp.n;
  if (helper) load_q(c.qn, qrow, sp.b2);
  if ((unim >> me) & 1) zero_q(c.qf);
  const __amdgpu_buffer_rsrc_t srdP = __builtin_amdgcn_make_buffer_rsrc((void*)(ws + PO_OFF), (short)0, (int)PO_BYTES, 0x00020000);
  c.lane = lane; c.full = 0; c.fulla = lds0 + FULL_OFF; c.donea = lds0 + DONE_OFF + 4 * w;
  c.kaddr = lds0 + ch * 256 + ((g ^ ch) << 4);
  {
    const unsigned vr = 4 * g + (ch >> 2), p = lane & 3;
    c.vaddr = lds0 + VOFF + vr * 256 + (((p >> 1) ^ (2 * (vr & 7))) << 4) + 8 * (p & 1);
  }
  const int nv0 = helper ? ((rl(len, me) - 1) & 63) + 1 : 64;
  TAKE(1);
#pragma unroll
  for (int k = 0; k < 8; ++k) c.o[k] = f32x4{0.f, 0.f, 0.f, 0.f};
  c.ones = __builtin_bit_cast(bf16x8, u32x4{0x3f803f80u, 0x3f803f80u, 0x3f803f80u, 0x3f803f80u});
  asm volatile("" : "+v"(c.ones), "+v"(c.donea) : "s"(out), "s"(srdP));
  c.l = f32x4{0.f, 0.f, 0.f, 0.f};
  c.m = -__builtin_inff();
  if (n > 0) { acquire(c, 0); step<false, true>(c, 0, nv0); retire(c, 0); }
  if ((unim >> sp.b2) & 1) zero_q(c.qn);
  asm volatile("" : "+v"(c.qn[0]), "+v"(c.qn[1]), "+v"(c.qn[2]), "+v"(c.qn[3]));
  TAKE(2);
  for (int i = 1; i < n;) {
    for (const int e = i <= n0 ? n0 : n; i < e; ++i) { acquire(c, i); step<true, false>(c, i, 64); retire(c, i); }
    if (i < n) { acquire(c, i); step<true, true>(c, i, 64); retire(c, i); ++i; }
  }
  TAKE(3);
  const unsigned so0 = (unsigned)((me * 8 + h) * 2) * PO_SLOT + w * PO_WAVE, so1 = (unsigned)(((helper ? sp.b2 : me) * 8 + h) * 2 + 1) * PO_SLOT + w * PO_WAVE;
  if (helper) {
    if (n > n0) store_part(c.held, srdP, lane, so0);
    acquire(c, n);
    TAKE(4);
    bf16x8 vf[8][2];
    read_v(c, n - 1, vf);
    pv(c, vf);
    Part p;
    pack_part(p, c.o, c.m, c.l[0]);
    store_part(p, srdP, lane, n > n0 ? so1 : so0);
    wait_vm<0>();
    bar();
    TAKE(7); FLUSH_STAMPS();
    return;
  }
  acquire(c, n + 1);
  TAKE(4);
  Part p0, p1;
  load_part(p0, srdP, lane, so0);
  load_part(p1, srdP, lane, sp.dep2 ? so1 : PO_BYTES);
  __builtin_amdgcn_sched_barrier(0);
  if (n > 0) {
    bf16x8 vf[8][2];
    read_v(c, n - 1, vf);
    pv(c, vf);
  }
  __builtin_amdgcn_sched_barrier(0);
  TAKE(5);
  {
    const float m0 = __uint_as_float(p0.ml[0]), m1 = sp.dep2 ? __uint_as_float(p1.ml[0]) : -__builtin_inff();
    const float mn = MX(MX(c.m, m0), m1);
    float a0 = __builtin_amdgcn_exp2f(c.m - mn), w0 = __builtin_amdgcn_exp2f(m0 - mn), w1 = __builtin_amdgcn_exp2f(m1 - mn);
    const float inv = __builtin_amdgcn_rcpf(c.l[0] * a0 + w0 * __uint_as_float(p0.ml[1]) + w1 * __uint_as_float(p1.ml[1]));
    a0 *= inv; w0 *= inv; w1 *= inv;
#pragma unroll
    for (int k = 0; k < 8; ++k) c.o[k] = c.o[k] * a0 + w0 * part_o(p0, k) + w1 * part_o(p1, k);
  }
  TAKE(6);
  const __amdgpu_buffer_rsrc_t srdO = __builtin_amdgcn_make_buffer_rsrc((void*)out, (short)0, 16 * HQ * SQ * HD * 2, 0x00020000);
  char* orow = out + ((size_t)((me * HQ + 4 * h + w) * SQ + ch) * HD + 16 * (g & 1) + 8 * (g >> 1)) * 2;
#pragma unroll
  for (int k = 0; k < 8; k += 2) {
    const unsigned a0 = pk2(c.o[k][0], c.o[k][1]), a1 = pk2(c.o[k][2], c.o[k][3]);
    const unsigned b0 = pk2(c.o[k + 1][0], c.o[k + 1][1]), b1 = pk2(c.o[k + 1][2], c.o[k + 1][3]);
    auto r0 = __builtin_amdgcn_permlane16_swap(a0, b0, false, false);
    auto r1 = __builtin_amdgcn_permlane16_swap(a1, b1, false, false);
    *(u32x4*)(orow + k * 32) = u32x4{r0[0], r1[0], r0[1], r1[1]};
  }
  TAKE(7); FLUSH_STAMPS();
}

}

extern "C" void kernel_launch(void* const* d_in, const int* in_sizes, int n_in, void* d_out, int out_size, void* d_ws, size_t ws_size,
                              hipStream_t stream) {
  (void)n_in; (void)out_size; (void)ws_size;
  hipLaunchKernelGGL(paged_attn_kernel, dim3(256), dim3(320), 0, stream, (const char*)d_in[0], (const char*)d_in[1], (const char*)d_in[2],
                     (const int*)d_in[3], (const int*)d_in[4], (char*)d_out, (char*)d_ws, (unsigned)in_sizes[1] * 2u, 0u);
}
```

```cpp
#include <hip/hip_runtime.h>
#include <stdint.h>

namespace {

typedef __attribute__((ext_vector_type(8))) __bf16 bf16x8;
typedef __attribute__((ext_vector_type(4))) __bf16 bf16x4;
typedef __attribute__((ext_vector_type(2))) __bf16 bf16x2;
typedef __attribute__((ext_vector_type(4))) float f32x4;
typedef __attribute__((ext_vector_type(2))) float f32x2;
typedef __attribute__((ext_vector_type(4))) unsigned u32x4;
typedef __attribute__((ext_vector_type(2))) unsigned u32x2;
typedef unsigned long long u64;
#define LAS __attribute__((address_space(3)))
#define GAS __attribute__((address_space(1)))

constexpr int HQ = 32, SQ = 16, HD = 128, NBPS = 256, SKV = 4096;
constexpr int XP = 8, XE = 7;
constexpr int W0 = 24, WEND = 32;
constexpr int NROUND = 3, SF0 = 30, SFD = 22, SDELAY = 170, NREF = 8;
constexpr float SGAIN = 1.2f, SCLAMP = 0.12f;
constexpr float THR = 12.f;
constexpr int CODE_BYTES = 12288;
constexpr int NS = 4;
constexpr unsigned SLOT = 32768, VOFF = 16384, DONE_OFF = NS * SLOT, FULL_OFF = DONE_OFF + 16, LDS_BYTES = FULL_OFF + 16;
constexpr unsigned REC_STRIDE = 64, PO_OFF = 65536, PO_WAVE = 4096 + 512, PO_BYTES = 1024 * PO_WAVE, PROG_STRIDE = 1280;
constexpr u64 TAG_STEP = 0x9E3779B97F4A7C15ull;
constexpr float CSCALE = 0.08838834764831845f * 1.4426950408889634f;
constexpr unsigned SPIN_CAP = 1u << 20;

#define MX(a, b) __builtin_elementwise_maximum((a), (b))
__device__ __forceinline__ int rl(int v, int l) { return __builtin_amdgcn_readlane(v, l); }
__device__ __forceinline__ int scan16(int x) {
  x += __builtin_amdgcn_update_dpp(0, x, 0x111, 0xf, 0xf, false);
  x += __builtin_amdgcn_update_dpp(0, x, 0x112, 0xf, 0xf, false);
  x += __builtin_amdgcn_update_dpp(0, x, 0x114, 0xf, 0xf, false);
  x += __builtin_amdgcn_update_dpp(0, x, 0x118, 0xf, 0xf, false);
  return x;
}
template <int R> __device__ __forceinline__ unsigned less16(unsigned key) {
  if constexpr (R == 0) return 0u;
  else return __builtin_amdgcn_alignbit(less16<R - 1>(key), (unsigned)__builtin_amdgcn_mov_dpp((int)key, 0x120 + R, 0xf, 0xf, true) - key, 31);
}
__device__ __forceinline__ float xmax(float x) {
  auto s = __builtin_amdgcn_permlane16_swap(__float_as_uint(x), __float_as_uint(x), false, false);
  x = MX(__uint_as_float(s[0]), __uint_as_float(s[1]));
  auto t = __builtin_amdgcn_permlane32_swap(__float_as_uint(x), __float_as_uint(x), false, false);
  return MX(__uint_as_float(t[0]), __uint_as_float(t[1]));
}
__device__ __forceinline__ unsigned pk2(float a, float b) {
  f32x2 v = {a, b};
  return __builtin_bit_cast(unsigned, __builtin_convertvector(v, bf16x2));
}
__device__ __forceinline__ unsigned now32() { return (unsigned)__builtin_amdgcn_s_memrealtime(); }

__device__ __forceinline__ void dma_page(unsigned ldsdst, u32x4 voff, __amdgpu_buffer_rsrc_t srd, unsigned soff) {
  asm volatile("s_mov_b32 m0, %0\n\ts_nop 3\n\tbuffer_load_dwordx4 %1, %5, %6 offen nt lds\n\t"
               "buffer_load_dwordx4 %2, %5, %6 offen offset:1024 nt lds\n\t"
               "buffer_load_dwordx4 %3, %5, %6 offen offset:2048 nt lds\n\t"
               "buffer_load_dwordx4 %4, %5, %6 offen offset:3072 nt lds"
               :: "s"(ldsdst), "v"(voff[0]), "v"(voff[1]), "v"(voff[2]), "v"(voff[3]), "s"(srd), "s"(soff) : "memory");
}
#define WT1(k) "s_waitcnt vmcnt(" #k ")\n\ts_setpc_b64 s[60:61]\n\t"
#define WT8(a, b, c, d, e, f, g, h) WT1(a) WT1(b) WT1(c) WT1(d) WT1(e) WT1(f) WT1(g) WT1(h)
__device__ __forceinline__ u64 wait_tab() {
  u64 a;
  asm volatile("s_getpc_b64 %0\n\ts_branch .Lwt_end%=\n\t"
               WT8(0, 1, 2, 3, 4, 5, 6, 7) WT8(8, 9, 10, 11, 12, 13, 14, 15) WT8(16, 17, 18, 19, 20, 21, 22, 23) WT8(24, 25, 26, 27, 28, 29, 30, 31)
               WT8(32, 33, 34, 35, 36, 37, 38, 39) WT8(40, 41, 42, 43, 44, 45, 46, 47) WT8(48, 49, 50, 51, 52, 53, 54, 55) WT8(56, 57, 58, 59, 60, 61, 62, 63)
               ".Lwt_end%=:" : "=s"(a));
  return a + 4;
}
__device__ __forceinline__ void wait_dyn(u64 entry) { asm volatile("s_swappc_b64 s[60:61], %0" :: "s"(entry) : "memory", "s60", "s61"); }
__device__ __forceinline__ void lds_bar() { asm volatile("s_waitcnt lgkmcnt(0)\n\ts_barrier" ::: "memory"); }
__device__ __forceinline__ void lds_put(unsigned addr, int v) { asm volatile("ds_write_b32 %0, %1" :: "v"(addr), "v"(v) : "memory"); }
__device__ __forceinline__ int lds_get(unsigned addr) {
  int v;
  asm volatile("ds_read_b32 %0, %1\n\ts_waitcnt lgkmcnt(0)" : "=v"(v) : "v"(addr) : "memory");
  return __builtin_amdgcn_readfirstlane(v);
}
__device__ __forceinline__ int lds_min4(unsigned addr) {
  u32x4 v;
  asm volatile("ds_read_b128 %0, %1\n\ts_waitcnt lgkmcnt(0)" : "=v"(v) : "v"(addr) : "memory");
  return __builtin_amdgcn_readfirstlane(min(min(v[0], v[1]), min(v[2], v[3])));
}

struct Part { u32x4 o[4]; u32x2 ml; };
__device__ __forceinline__ void pack_part(Part& p, const f32x4 (&o)[8], float m, float l) {
#pragma unroll
  for (int k = 0; k < 4; ++k) p.o[k] = u32x4{pk2(o[2 * k][0], o[2 * k][1]), pk2(o[2 * k][2], o[2 * k][3]), pk2(o[2 * k + 1][0], o[2 * k + 1][1]), pk2(o[2 * k + 1][2], o[2 * k + 1][3])};
  p.ml = u32x2{__float_as_uint(m), __float_as_uint(l)};
}
__device__ __forceinline__ f32x4 part_o(const Part& p, int k) {
  const unsigned a = p.o[k >> 1][2 * (k & 1)], b = p.o[k >> 1][2 * (k & 1) + 1];
  return f32x4{__uint_as_float(a << 16), __uint_as_float(a & 0xffff0000u), __uint_as_float(b << 16), __uint_as_float(b & 0xffff0000u)};
}
__device__ __forceinline__ void store_part(const Part& p, __amdgpu_buffer_rsrc_t srd, int lane, unsigned so) {
#pragma unroll
  for (int k = 0; k < 4; ++k) __builtin_amdgcn_raw_buffer_store_b128(p.o[k], srd, lane * 16 + k * 1024, so, 16);
  __builtin_amdgcn_raw_buffer_store_b64(p.ml, srd, lane * 8, so + 4096, 16);
}
__device__ __forceinline__ void request_parts(__amdgpu_buffer_rsrc_t srd, int lane, unsigned sa, unsigned sb) {
  asm volatile("s_nop 4\n\tbuffer_load_dwordx2 v[232:233], %1, %2, %4 offen sc1\n\tbuffer_load_dwordx2 v[250:251], %1, %2, %6 offen sc1\n\t"
               "buffer_load_dwordx4 v[216:219], %0, %2, %3 offen sc1\n\tbuffer_load_dwordx4 v[234:237], %0, %2, %5 offen sc1\n\t"
               "buffer_load_dwordx4 v[220:223], %0, %2, %3 offen offset:1024 sc1\n\tbuffer_load_dwordx4 v[238:241], %0, %2, %5 offen offset:1024 sc1\n\t"
               "buffer_load_dwordx4 v[224:227], %0, %2, %3 offen offset:2048 sc1\n\tbuffer_load_dwordx4 v[242:245], %0, %2, %5 offen offset:2048 sc1\n\t"
               "buffer_load_dwordx4 v[228:231], %0, %2, %3 offen offset:3072 sc1\n\tbuffer_load_dwordx4 v[246:249], %0, %2, %5 offen offset:3072 sc1"
               :: "v"(lane * 16), "v"(lane * 8), "s"(srd), "s"(sa), "s"(sa + 4096), "s"(sb), "s"(sb + 4096)
               : "memory", "v216", "v217", "v218", "v219", "v220", "v221", "v222", "v223", "v224", "v225", "v226", "v227", "v228", "v229", "v230", "v231", "v232", "v233",
                 "v234", "v235", "v236", "v237", "v238", "v239", "v240", "v241", "v242", "v243", "v244", "v245", "v246", "v247", "v248", "v249", "v250", "v251");
}
__device__ __forceinline__ void take_parts(Part& a, Part& b) {
  asm volatile("s_waitcnt vmcnt(0)" : "={v[216:219]}"(a.o[0]), "={v[220:223]}"(a.o[1]), "={v[224:227]}"(a.o[2]), "={v[228:231]}"(a.o[3]), "={v[232:233]}"(a.ml),
               "={v[234:237]}"(b.o[0]), "={v[238:241]}"(b.o[1]), "={v[242:245]}"(b.o[2]), "={v[246:249]}"(b.o[3]), "={v[250:251]}"(b.ml) :: "memory");
}
__device__ __forceinline__ void request_flag(GAS char* rec) { asm volatile("global_load_dwordx2 v[254:255], %0, off offset:8 sc1" :: "v"(rec) : "memory", "v254", "v255"); }
__device__ __forceinline__ u64 take_flag() {
  u64 f;
  asm volatile("s_waitcnt vmcnt(0)" : "={v[254:255]}"(f) :: "memory");
  return f;
}

struct Split { int n0, n, b2, t2, dep2, pages, wfix; };
__device__ __forceinline__ Split split_work(int len, int me, bool helper) {
  const int P = (len + 15) >> 4, rank = __builtin_popcount(less16<15>(P * 16 + (threadIdx.x & 15)));
  const int tot = rl(scan16(P), 15);
  const int rk = rl(rank, me), other = __builtin_ctz((unsigned)__builtin_amdgcn_ballot_w64(rank == 15 - rk));
  const bool isL = rk >= 8;
  const int Pm = rl(P, me), Po = rl(P, other), L = isL ? Pm : Po, S = isL ? Po : Pm, tL = (L + 3) >> 2, tS = (S + 3) >> 2;
  const int a2 = (L + S + 2 * XP + 4) >> 3;
  const int aS = min(a2 >> 1, tS - 1);
  int aL = a2 - (a2 >> 1);
  if (aS < (a2 >> 1)) aL = (int)(((unsigned)(L + S - 4 * aS + 2 * XP + 6) * 21846u) >> 18);
  aL = min(aL, tL - 1);
  const int R = L + S - 4 * (aL + aS);
  const int kL = max(aL, min(tL - 1, (2 * L - R + 4) >> 3));
  Split r;
  r.b2 = other; r.t2 = kL; r.dep2 = isL && kL > aL;
  if (!helper) { r.n0 = r.n = isL ? aL : aS; r.pages = 4 * r.n - XE; }
  else if (isL) { r.n0 = r.n = tL - kL; r.pages = L - 4 * kL; }
  else { r.n0 = tS - aS; r.n = r.n0 + kL - aL; r.pages = S - 4 * aS + 4 * (kL - aL); }
  const int share = (int)((float)(r.pages * 32 * 256) * __builtin_amdgcn_rcpf((float)max(tot - 16 * XE, 16)));
  r.wfix = __builtin_amdgcn_readfirstlane(min(max(2 * 256 + (W0 - 2) * share, 12 * 256), 48 * 256));
  return r;
}
__device__ __forceinline__ void load_q(bf16x8 (&qf)[4], const char* qrow, int b) {
#pragma unroll
  for (int s = 0; s < 4; ++s) qf[s] = *(const bf16x8*)(qrow + (size_t)b * (HQ * SQ * HD * 2) + s * 64);
}
__device__ __forceinline__ void zero_q(bf16x8 (&qf)[4]) {
#pragma unroll
  for (int s = 0; s < 4; ++s) qf[s] = __builtin_bit_cast(bf16x8, u32x4{0u, 0u, 0u, 0u});
}

__global__ __launch_bounds__(384) void paged_attn_kernel(const char* __restrict__ q, const char* __restrict__ kc, const char* __restrict__ vc,
                                                        const int* __restrict__ bt, const int* __restrict__ lens, char* __restrict__ out,
                                                        char* ws, unsigned kvbytes, unsigned pad_) {
  __shared__ __attribute__((aligned(1024))) char lds[LDS_BYTES];
  {
    const unsigned line = (blockIdx.x >> 3) * 6 + threadIdx.x;
    u64 pc0;
    asm volatile("s_getpc_b64 %0" : "=s"(pc0));
    if (threadIdx.x < 6) asm volatile("global_load_dword v252, %0, %1" :: "v"(line % (CODE_BYTES / 128) * 128), "s"(pc0 & ~255ull) : "memory", "v252");
  }
  const int bid = blockIdx.x, lane = threadIdx.x & 63, w = __builtin_amdgcn_readfirstlane(threadIdx.x >> 6);
  const bool helper = bid >= 128;
  const int h = bid & 7, me = (bid >> 3) & 15;
  const unsigned lds0 = (unsigned)(size_t)(LAS char*)lds;
  const unsigned g = lane >> 4, ch = lane & 15;
  lds_put(lds0 + DONE_OFF + 4 * w, 0);

  if (__builtin_expect(w == 4, 1)) {
    __builtin_amdgcn_s_setprio(3);
    u32x4 pg = *(const u32x4*)(bt + me * NBPS + 4 * lane);
    int len = lens[ch];
    __builtin_amdgcn_sched_barrier(0);
    lds_bar();
    const __amdgpu_buffer_rsrc_t srdK = __builtin_amdgcn_make_buffer_rsrc((void*)kc, (short)0, (int)kvbytes, 0x00020000);
    const __amdgpu_buffer_rsrc_t srdV = __builtin_amdgcn_make_buffer_rsrc((void*)vc, (short)0, (int)kvbytes, 0x00020000);
    u32x4 vk, vv;
#pragma unroll
    for (int i = 0; i < 4; ++i) {
      const unsigned row = 4 * i + g;
      vk[i] = row * 2048 + h * 256 + ((ch ^ row) << 4) - i * 1024;
      vv[i] = row * 2048 + h * 256 + ((ch ^ (2 * (row & 7))) << 4) - i * 1024;
    }
    asm volatile("" : "+v"(vk), "+v"(vv) : "s"(srdK), "s"(srdV));
    __builtin_amdgcn_sched_barrier(0);
    len = len <= 0 ? SKV : min(len, SKV);
    pg <<= 15;
    const int Pm = (rl(len, me) + 15) >> 4, tf = helper ? (Pm - 1) >> 2 : 0, cut0 = helper ? 4 * (Pm - 4 * tf) : 16;
    unsigned p0 = rl(pg[0], tf), p1 = rl(pg[1], tf), p2 = rl(pg[2], tf), p3 = rl(pg[3], tf);
    dma_page(lds0, vk, srdK, p0);
    if (cut0 > 4) dma_page(lds0 + 4096, vk, srdK, p1);
    if (cut0 > 8) dma_page(lds0 + 8192, vk, srdK, p2);
    if (cut0 > 12) dma_page(lds0 + 12288, vk, srdK, p3);
    dma_page(lds0 + VOFF, vv, srdV, p0);
    if (cut0 > 4) dma_page(lds0 + VOFF + 4096, vv, srdV, p1);
    if (cut0 > 8) dma_page(lds0 + VOFF + 8192, vv, srdV, p2);
    if (cut0 > 12) dma_page(lds0 + VOFF + 12288, vv, srdV, p3);
    __builtin_amdgcn_sched_barrier(0);
    for (int k = cut0; k < 16; ++k) *(LAS u32x4*)(size_t)(lds0 + VOFF + k * 1024 + lane * 16) = u32x4{0u, 0u, 0u, 0u};
    const Split sp = split_work(len, me, helper);
    const int n0 = sp.n0, n = sp.n;
    asm volatile("global_load_dwordx4 v[224:227], %0, off" :: "v"(bt + sp.b2 * NBPS + 4 * lane) : "memory", "v224", "v225", "v226", "v227");
    const unsigned fulla = lds0 + FULL_OFF, donea = lds0 + DONE_OFF;
    const int tend = helper ? n : n - (XE + 3) / 4;
    const u64 tab = wait_tab();
    const int mark = 2 * cut0;
    int acc = 128, wadj = 0, issued = mark, rel = 0, hend = cut0, freed = 0, tl = tf;
    auto release = [&]() { hend += rel ? 16 : cut0; ++rel; lds_put(fulla, rel); };
    for (int t = 1; t < n; ++t) {
      acc = (acc & 255) + sp.wfix + wadj;
      const int win = t >= tend ? WEND : acc >> 8;
      const u64 ent = tab + 8 * (win - 4);
      tl += helper ? -1 : 1;
      if (t == n0) {
        wait_dyn(tab + 8 * min(issued - mark, 63));
        asm volatile("" : "={v[224:227]}"(pg));
        pg <<= 15;
        tl = sp.t2 - 1;
      }
      const unsigned dst = lds0 + (t & (NS - 1)) * SLOT;
      p0 = rl(pg[0], tl); p1 = rl(pg[1], tl); p2 = rl(pg[2], tl); p3 = rl(pg[3], tl);
      for (unsigned spins = 0; freed <= t - NS && spins < SPIN_CAP; ++spins) freed = lds_min4(donea);
#define CHUNK(dst, vo, srd, p) { wait_dyn(ent); dma_page(dst, vo, srd, p); if (issued - win + 4 >= hend) release(); issued += 4; }
      CHUNK(dst, vk, srdK, p0) CHUNK(dst + 4096, vk, srdK, p1) CHUNK(dst + 8192, vk, srdK, p2) CHUNK(dst + 12288, vk, srdK, p3)
      CHUNK(dst + VOFF, vv, srdV, p0) CHUNK(dst + VOFF + 4096, vv, srdV, p1) CHUNK(dst + VOFF + 8192, vv, srdV, p2)
      asm volatile("ds_read_b128 v[216:219], %0\n\tds_read_b32 v220, %0 offset:20" :: "v"(donea) : "memory", "v216", "v217", "v218", "v219", "v220");
      CHUNK(dst + VOFF + 12288, vv, srdV, p3)
#undef CHUNK
      u32x4 d4; int adj;
      asm volatile("s_waitcnt lgkmcnt(0)" : "={v[216:219]}"(d4), "={v220}"(adj) :: "memory");
      freed = __builtin_amdgcn_readfirstlane(min(min(d4[0], d4[1]), min(d4[2], d4[3])));
      wadj = __builtin_amdgcn_readfirstlane(min(max(adj, -4 * 256), 8 * 256));
    }
    while (rel < 2 * n) { wait_dyn(tab + 8 * min(issued - hend, 63)); release(); }
    asm volatile("s_waitcnt vmcnt(0)" ::: "memory");
    return;
  }

  const char* qrow = q + ((4 * h + w) * SQ + ch) * (HD * 2) + g * 16;
  bf16x8 qf[4], qn[4];
  lds_bar();
  __builtin_amdgcn_sched_barrier(0);
  int len = lens[ch];
  if (w < 4) load_q(qf, qrow, me);
  const unsigned unim = (unsigned)__builtin_amdgcn_ballot_w64(len <= 0);
  len = len <= 0 ? SKV : min(len, SKV);
  const Split sp = split_work(len, me, helper);
  const int n0 = sp.n0, n = sp.n;

  if (__builtin_expect(w == 5, 0)) {
    const int Pm = (rl(len, me) + 15) >> 4, cut0 = helper ? 4 * (((Pm - 1) & 3) + 1) : 16, steady = 8 * sp.pages;
    if (sp.pages < 48) return;
    auto reach = [&](int j) {
      for (unsigned spins = 0; spins < SPIN_CAP && lds_get(lds0 + FULL_OFF) < j; ++spins) __builtin_amdgcn_s_sleep(16);
      for (unsigned spins = 0; spins < SPIN_CAP && lds_get(lds0 + FULL_OFF) <= j; ++spins) __builtin_amdgcn_s_sleep(1);
      return now32();
    };
    const __amdgpu_buffer_rsrc_t srdG = __builtin_amdgcn_make_buffer_rsrc((void*)(ws + PO_OFF + PO_BYTES), (short)0, NROUND * NREF * PROG_STRIDE, 0x00020000);
    const int rk = bid / (256 / NREF), ref = bid == rk * (256 / NREF) + (rk * 11 + 2) % (256 / NREF) ? rk : -1;
    const unsigned tz = reach(3);
    float fprev = 0.f;
#pragma unroll 1
    for (int r = 0; r < NROUND; ++r) {
      const int goal = steady * (SF0 + r * SFD) / 100, j = (goal - 2 * cut0 + 8) / 16 + 1, hend = 2 * cut0 + 16 * (j - 1);
      const unsigned now = reach(j);
      const float tpl = (float)(int)(now - tz) * __builtin_amdgcn_rcpf((float)(16 * (j - 3)));
      const unsigned stamp = now + (int)((float)(goal - hend) * tpl);
      const unsigned po = r * NREF * PROG_STRIDE;
      if (ref >= 0) __builtin_amdgcn_raw_buffer_store_b128(u32x4{stamp, stamp, stamp, stamp}, srdG, lane * 16, po + ref * PROG_STRIDE, 16);
      for (unsigned spins = 0; spins < 4096 && (int)(now32() - stamp) < SDELAY; ++spins) __builtin_amdgcn_s_sleep(4);
      const int d = (int)(stamp - __builtin_amdgcn_raw_buffer_load_b32(srdG, (ch & (NREF - 1)) * PROG_STRIDE + (bid & 7) * 128, po, 16));
      const int tot = rl(scan16((unsigned)(d + 400) < 800u ? d * 1024 + 1 : 0), NREF - 1);
      const float since = (float)(int)(now32() - stamp) + 60.f;
      const float lag = (float)(tot >> 10) * __builtin_amdgcn_rcpf((float)max(tot & 1023, 1)) - fprev * since;
      const float span = r + 1 < NROUND ? tpl * (float)(steady * SFD / 100) : fmaxf(tpl * (float)(steady - goal) - since, 100.f);
      fprev = fminf(fmaxf(lag * __builtin_amdgcn_rcpf(span), -SCLAMP), SCLAMP);
      lds_put(lds0 + FULL_OFF + 4, (int)(SGAIN * (float)(sp.wfix - 512) * fprev));
    }
    return;
  }

  const unsigned slot0 = (unsigned)((me * 8 + h) * 2) * 4 + w, slot1 = (unsigned)(((helper ? sp.b2 : me) * 8 + h) * 2 + 1) * 4 + w;
  GAS char* rec = (GAS char*)ws + (lane & 1 ? slot1 : slot0) * REC_STRIDE;
  const bool two = helper ? n > n0 : sp.dep2, mine = !(lane & 1) || two;
  u64 tag = __hip_atomic_load((GAS u64*)rec, __ATOMIC_RELAXED, __HIP_MEMORY_SCOPE_AGENT) + TAG_STEP;
  if (n > n0 && !((unim >> sp.b2) & 1)) load_q(qn, qrow, sp.b2); else zero_q(qn);
  if ((unim >> me) & 1) zero_q(qf);
  const __amdgpu_buffer_rsrc_t srdP = __builtin_amdgcn_make_buffer_rsrc((void*)(ws + PO_OFF), (short)0, (int)PO_BYTES, 0x00020000);
  const __amdgpu_buffer_rsrc_t srdO = __builtin_amdgcn_make_buffer_rsrc((void*)out, (short)0, 16 * HQ * SQ * HD * 2, 0x00020000);
  const unsigned kaddr = lds0 + ch * 256 + ((g ^ ch) << 4);
  const unsigned vaddr = lds0 + VOFF + (4 * g + (ch >> 2)) * 256 + ((((lane >> 1) & 1) ^ (2 * ((4 * g + (ch >> 2)) & 7))) << 4) + 8 * (lane & 1);
  unsigned fulla = lds0 + FULL_OFF, donea = lds0 + DONE_OFF + 4 * w;
  int nv = helper ? ((rl(len, me) - 1) & 63) + 1 : 64;
  bf16x8 ones = __builtin_bit_cast(bf16x8, u32x4{0x3f803f80u, 0x3f803f80u, 0x3f803f80u, 0x3f803f80u});
  f32x4 o[8], l = {0.f, 0.f, 0.f, 0.f};
  float m = -__builtin_inff();
#pragma unroll
  for (int k = 0; k < 8; ++k) o[k] = f32x4{0.f, 0.f, 0.f, 0.f};
  Part held = {};
  asm volatile("" : "+v"(ones), "+v"(fulla), "+v"(donea), "+v"(tag), "+v"(qf[0]), "+v"(qf[1]), "+v"(qf[2]), "+v"(qf[3]), "+v"(qn[0]), "+v"(qn[1]), "+v"(qn[2]), "+v"(qn[3])
               : "s"(srdP), "s"(srdO));
  int full = 0, pst = 0;
  auto acquire = [&](int j) {
    for (unsigned spins = 0; full <= j && spins < SPIN_CAP; ++spins) {
      full = lds_get(fulla);
      if (full <= j) __builtin_amdgcn_s_sleep(1);
    }
    asm volatile("" ::: "memory");
  };
  auto poll = [&]() {
    if (pst == 1 && __builtin_amdgcn_ballot_w64(take_flag() != tag && mine) == 0) {
      request_parts(srdP, lane, slot0 * PO_WAVE, two ? slot1 * PO_WAVE : PO_BYTES);
      pst = 2;
    }
    if (pst < 2) { request_flag(rec); pst = 1; }
  };
  const int ifrom = helper ? n0 : n - 1;
  for (int i = 0; i < n; ++i) {
    if (__builtin_expect(i >= ifrom, 0)) {
      if (!helper) poll();
      else if (i == n0) {
        pack_part(held, o, m, l[0]);
#pragma unroll
        for (int k = 0; k < 8; ++k) o[k] = f32x4{0.f, 0.f, 0.f, 0.f};
        l = f32x4{0.f, 0.f, 0.f, 0.f};
        m = -__builtin_inff();
#pragma unroll
        for (int s = 0; s < 4; ++s) qf[s] = qn[s];
      }
    }
    acquire(2 * i);
    const unsigned so = (i & (NS - 1)) * SLOT;
    f32x4 s[4];
    {
      bf16x8 kf[4][4];
#pragma unroll
      for (int st = 0; st < 4; ++st)
#pragma unroll
        for (int kb = 0; kb < 4; ++kb) kf[kb][st] = *(LAS bf16x8*)(size_t)(((kaddr + so) ^ (st << 6)) + kb * 4096);
#pragma unroll
      for (int kb = 0; kb < 4; ++kb) s[kb] = f32x4{0.f, 0.f, 0.f, 0.f};
#pragma unroll
      for (int st = 0; st < 4; ++st)
#pragma unroll
        for (int kb = 0; kb < 4; ++kb) s[kb] = __builtin_amdgcn_mfma_f32_16x16x32_bf16(kf[kb][st], qf[st], s[kb], 0, 0, 0);
      __builtin_amdgcn_sched_group_barrier(0x2, 5, 1);
      __builtin_amdgcn_sched_group_barrier(0x100, 6, 1);
#pragma unroll
      for (int k = 0; k < 10; ++k) { __builtin_amdgcn_sched_group_barrier(0x8, 1, 1); __builtin_amdgcn_sched_group_barrier(0x100, 1, 1); }
      __builtin_amdgcn_sched_group_barrier(0x8, 6, 1);
    }
    if (__builtin_expect(nv < 64, 0)) {
      const int lim = nv - 4 * g;
#pragma unroll
      for (int kb = 0; kb < 4; ++kb)
#pragma unroll
        for (int r = 0; r < 4; ++r) s[kb][r] = (16 * kb + r < lim) ? s[kb][r] : -__builtin_inff();
      nv = 64;
    }
    const float t0 = MX(MX(MX(MX(s[0][0], s[0][1]), s[0][2]), s[0][3]), s[1][0]), t1 = MX(MX(MX(MX(s[1][1], s[1][2]), s[1][3]), s[2][0]), s[2][1]);
    const float t2 = MX(MX(MX(MX(s[2][2], s[2][3]), s[3][0]), s[3][1]), s[3][2]);
    const float t = MX(MX(MX(t0, t1), t2), s[3][3]);
    if (__builtin_expect(__builtin_amdgcn_ballot_w64(!(fmaf(t, CSCALE, -m) <= THR)) != 0, 0)) {
      const float mn = MX(m, xmax(t) * CSCALE), alpha = __builtin_amdgcn_exp2f(m - mn);
#pragma unroll
      for (int k = 0; k < 8; ++k) o[k] *= alpha;
      l *= alpha;
      m = mn;
    }
    bf16x8 pk[2];
#pragma unroll
    for (int kb = 0; kb < 4; ++kb)
#pragma unroll
      for (int r = 0; r < 4; ++r) s[kb][r] = __builtin_amdgcn_exp2f(fmaf(s[kb][r], CSCALE, -m));
#pragma unroll
    for (int u = 0; u < 2; ++u)
      pk[u] = __builtin_bit_cast(bf16x8, u32x4{pk2(s[2 * u][0], s[2 * u][1]), pk2(s[2 * u][2], s[2 * u][3]), pk2(s[2 * u + 1][0], s[2 * u + 1][1]), pk2(s[2 * u + 1][2], s[2 * u + 1][3])});
    l = __builtin_amdgcn_mfma_f32_16x16x32_bf16(ones, pk[0], l, 0, 0, 0);
    l = __builtin_amdgcn_mfma_f32_16x16x32_bf16(ones, pk[1], l, 0, 0, 0);
    asm volatile("" : "+v"(pk[0]), "+v"(pk[1]));
    if (__builtin_expect(i >= ifrom, 0) && !helper) poll();
    acquire(2 * i + 1);
    {
      bf16x8 vf[8][2];
#pragma unroll
      for (int k = 0; k < 8; ++k) {
        const unsigned a = (vaddr + so) ^ (k << 5);
#pragma unroll
        for (int u = 0; u < 2; ++u) {
          const bf16x4 lo = __builtin_amdgcn_ds_read_tr16_b64_v4bf16((LAS bf16x4*)(size_t)(a + u * 8192));
          const bf16x4 hi = __builtin_amdgcn_ds_read_tr16_b64_v4bf16((LAS bf16x4*)(size_t)(a + u * 8192 + 4096));
          vf[k][u] = __builtin_shufflevector(lo, hi, 0, 1, 2, 3, 4, 5, 6, 7);
        }
      }
#pragma unroll
      for (int k = 0; k < 8; ++k)
#pragma unroll
        for (int u = 0; u < 2; ++u) o[k] = __builtin_amdgcn_mfma_f32_16x16x32_bf16(vf[k][u], pk[u], o[k], 0, 0, 0);
      __builtin_amdgcn_sched_group_barrier(0x2, 8, 0);
      __builtin_amdgcn_sched_group_barrier(0x100, 12, 0);
#pragma unroll
      for (int k = 0; k < 10; ++k) { __builtin_amdgcn_sched_group_barrier(0x8, 1, 0); __builtin_amdgcn_sched_group_barrier(0x100, 2, 0); }
      __builtin_amdgcn_sched_group_barrier(0x8, 6, 0);
    }
    asm volatile("s_waitcnt lgkmcnt(0)\n\tds_write_b32 %0, %1" :: "v"(donea), "v"(i + 1) : "memory");
  }
  if (helper) {
    if (two) store_part(held, srdP, lane, slot0 * PO_WAVE);
    pack_part(held, o, m, l[0]);
    store_part(held, srdP, lane, (two ? slot1 : slot0) * PO_WAVE);
    asm volatile("s_waitcnt vmcnt(0)" ::: "memory");
    if (lane < 2 && mine) __hip_atomic_store((GAS u64*)(rec + 8), tag, __ATOMIC_RELAXED, __HIP_MEMORY_SCOPE_AGENT);
    return;
  }
  for (unsigned spins = 0; pst < 2 && spins < SPIN_CAP; ++spins) poll();
  Part pa, pb;
  take_parts(pa, pb);
  {
    const float m0 = __uint_as_float(pa.ml[0]), m1 = two ? __uint_as_float(pb.ml[0]) : -__builtin_inff();
    const float mn = MX(MX(m, m0), m1);
    float a0 = __builtin_amdgcn_exp2f(m - mn), w0 = __builtin_amdgcn_exp2f(m0 - mn), w1 = __builtin_amdgcn_exp2f(m1 - mn);
    const float inv = __builtin_amdgcn_rcpf(l[0] * a0 + w0 * __uint_as_float(pa.ml[1]) + w1 * __uint_as_float(pb.ml[1]));
    a0 *= inv; w0 *= inv; w1 *= inv;
    const unsigned oo = (((me * HQ + 4 * h + w) * SQ + ch) * HD + 16 * (g & 1) + 8 * (g >> 1)) * 2;
#pragma unroll
    for (int k = 0; k < 8; k += 2) {
      const f32x4 x = o[k] * a0 + w0 * part_o(pa, k) + w1 * part_o(pb, k), y = o[k + 1] * a0 + w0 * part_o(pa, k + 1) + w1 * part_o(pb, k + 1);
      auto r0 = __builtin_amdgcn_permlane16_swap(pk2(x[0], x[1]), pk2(y[0], y[1]), false, false);
      auto r1 = __builtin_amdgcn_permlane16_swap(pk2(x[2], x[3]), pk2(y[2], y[3]), false, false);
      __builtin_amdgcn_raw_buffer_store_b128(u32x4{r0[0], r1[0], r0[1], r1[1]}, srdO, oo + k * 32, 0, 16);
    }
  }
  if (lane < 2 && mine) __hip_atomic_store((GAS u64*)rec, tag, __ATOMIC_RELAXED, __HIP_MEMORY_SCOPE_AGENT);
}

}

extern "C" void kernel_launch(void* const* d_in, const int* in_sizes, int n_in, void* d_out, int out_size, void* d_ws, size_t ws_size,
                              hipStream_t stream) {
  (void)n_in; (void)out_size; (void)ws_size;
  hipLaunchKernelGGL(paged_attn_kernel, dim3(256), dim3(384), 0, stream, (const char*)d_in[0], (const char*)d_in[1], (const char*)d_in[2],
                     (const int*)d_in[3], (const int*)d_in[4], (char*)d_out, (char*)d_ws, (unsigned)in_sizes[1] * 2u, 0u);
}
```

```cpp
#include <hip/hip_runtime.h>
#include <stdint.h>

namespace {

typedef __attribute__((ext_vector_type(8))) __bf16 bf16x8;
typedef __attribute__((ext_vector_type(4))) __bf16 bf16x4;
typedef __attribute__((ext_vector_type(2))) __bf16 bf16x2;
typedef __attribute__((ext_vector_type(4))) float f32x4;
typedef __attribute__((ext_vector_type(2))) float f32x2;
typedef __attribute__((ext_vector_type(4))) unsigned u32x4;
typedef __attribute__((ext_vector_type(2))) unsigned u32x2;
typedef unsigned long long u64;
#define LAS __attribute__((address_space(3)))
#define GAS __attribute__((address_space(1)))

constexpr int HQ = 32, SQ = 16, HD = 128, NBPS = 256, SKV = 4096;
constexpr int XP = 8, XE = 7;
constexpr int W0 = 24, WEND = 32;
constexpr int NROUND = 3, SF0 = 30, SFD = 22, SDELAY = 170, NREF = 4;
constexpr float SGAIN = 1.2f, SCLAMP = 0.12f;
constexpr float THR = 12.f;
constexpr int CODE_BYTES = 12288;
constexpr int NS = 4;
constexpr unsigned SLOT = 32768, VOFF = 16384, DONE_OFF = NS * SLOT, FULL_OFF = DONE_OFF + 16, LDS_BYTES = FULL_OFF + 16;
constexpr unsigned REC_STRIDE = 128, PO_OFF = 131072, PO_WAVE = 4096 + 128, PO_BYTES = 1024 * PO_WAVE, PROG_STRIDE = 1280;
constexpr u64 TAG_STEP = 0x9E3779B97F4A7C15ull;
constexpr float CSCALE = 0.08838834764831845f * 1.4426950408889634f;
constexpr unsigned SPIN_CAP = 1u << 20;

#define MX(a, b) __builtin_elementwise_maximum((a), (b))
__device__ __forceinline__ int rl(int v, int l) { return __builtin_amdgcn_readlane(v, l); }
__device__ __forceinline__ int scan16(int x) {
  x += __builtin_amdgcn_update_dpp(0, x, 0x111, 0xf, 0xf, false);
  x += __builtin_amdgcn_update_dpp(0, x, 0x112, 0xf, 0xf, false);
  x += __builtin_amdgcn_update_dpp(0, x, 0x114, 0xf, 0xf, false);
  x += __builtin_amdgcn_update_dpp(0, x, 0x118, 0xf, 0xf, false);
  return x;
}
template <int R> __device__ __forceinline__ unsigned less16(unsigned key) {
  if constexpr (R == 0) return 0u;
  else return __builtin_amdgcn_alignbit(less16<R - 1>(key), (unsigned)__builtin_amdgcn_mov_dpp((int)key, 0x120 + R, 0xf, 0xf, true) - key, 31);
}
__device__ __forceinline__ float xmax(float x) {
  auto s = __builtin_amdgcn_permlane16_swap(__float_as_uint(x), __float_as_uint(x), false, false);
  x = MX(__uint_as_float(s[0]), __uint_as_float(s[1]));
  auto t = __builtin_amdgcn_permlane32_swap(__float_as_uint(x), __float_as_uint(x), false, false);
  return MX(__uint_as_float(t[0]), __uint_as_float(t[1]));
}
__device__ __forceinline__ unsigned pk2(float a, float b) {
  f32x2 v = {a, b};
  return __builtin_bit_cast(unsigned, __builtin_convertvector(v, bf16x2));
}
__device__ __forceinline__ unsigned now32() { return (unsigned)__builtin_amdgcn_s_memrealtime(); }

__device__ __forceinline__ void dma_page(unsigned ldsdst, u32x4 voff, __amdgpu_buffer_rsrc_t srd, unsigned soff) {
  asm volatile("s_mov_b32 m0, %0\n\ts_nop 3\n\tbuffer_load_dwordx4 %1, %5, %6 offen nt lds\n\t"
               "buffer_load_dwordx4 %2, %5, %6 offen offset:1024 nt lds\n\t"
               "buffer_load_dwordx4 %3, %5, %6 offen offset:2048 nt lds\n\t"
               "buffer_load_dwordx4 %4, %5, %6 offen offset:3072 nt lds"
               :: "s"(ldsdst), "v"(voff[0]), "v"(voff[1]), "v"(voff[2]), "v"(voff[3]), "s"(srd), "s"(soff) : "memory");
}
#define WT1(k) "s_waitcnt vmcnt(" #k ")\n\ts_setpc_b64 s[60:61]\n\t"
#define WT8(a, b, c, d, e, f, g, h) WT1(a) WT1(b) WT1(c) WT1(d) WT1(e) WT1(f) WT1(g) WT1(h)
__device__ __forceinline__ u64 wait_tab() {
  u64 a;
  asm volatile("s_getpc_b64 %0\n\ts_branch .Lwt_end%=\n\t"
               WT8(0, 1, 2, 3, 4, 5, 6, 7) WT8(8, 9, 10, 11, 12, 13, 14, 15) WT8(16, 17, 18, 19, 20, 21, 22, 23) WT8(24, 25, 26, 27, 28, 29, 30, 31)
               WT8(32, 33, 34, 35, 36, 37, 38, 39) WT8(40, 41, 42, 43, 44, 45, 46, 47) WT8(48, 49, 50, 51, 52, 53, 54, 55) WT8(56, 57, 58, 59, 60, 61, 62, 63)
               ".Lwt_end%=:" : "=s"(a));
  return a + 4;
}
__device__ __forceinline__ void wait_dyn(u64 entry) { asm volatile("s_swappc_b64 s[60:61], %0" :: "s"(entry) : "memory", "s60", "s61"); }
__device__ __forceinline__ void lds_bar() { asm volatile("s_waitcnt lgkmcnt(0)\n\ts_barrier" ::: "memory"); }
__device__ __forceinline__ void lds_put(unsigned addr, int v) { asm volatile("ds_write_b32 %0, %1" :: "v"(addr), "v"(v) : "memory"); }
__device__ __forceinline__ int lds_get(unsigned addr) {
  int v;
  asm volatile("ds_read_b32 %0, %1\n\ts_waitcnt lgkmcnt(0)" : "=v"(v) : "v"(addr) : "memory");
  return __builtin_amdgcn_readfirstlane(v);
}
__device__ __forceinline__ int lds_min4(unsigned addr) {
  u32x4 v;
  asm volatile("ds_read_b128 %0, %1\n\ts_waitcnt lgkmcnt(0)" : "=v"(v) : "v"(addr) : "memory");
  return __builtin_amdgcn_readfirstlane(min(min(v[0], v[1]), min(v[2], v[3])));
}

struct Part { u32x4 o[4]; u32x2 ml; };
__device__ __forceinline__ void pack_part(Part& p, const f32x4 (&o)[8], float m, float l) {
#pragma unroll
  for (int k = 0; k < 4; ++k) p.o[k] = u32x4{pk2(o[2 * k][0], o[2 * k][1]), pk2(o[2 * k][2], o[2 * k][3]), pk2(o[2 * k + 1][0], o[2 * k + 1][1]), pk2(o[2 * k + 1][2], o[2 * k + 1][3])};
  p.ml = u32x2{__float_as_uint(m), __float_as_uint(l)};
}
__device__ __forceinline__ f32x4 part_o(const Part& p, int k) {
  const unsigned a = p.o[k >> 1][2 * (k & 1)], b = p.o[k >> 1][2 * (k & 1) + 1];
  return f32x4{__uint_as_float(a << 16), __uint_as_float(a & 0xffff0000u), __uint_as_float(b << 16), __uint_as_float(b & 0xffff0000u)};
}
__device__ __forceinline__ void store_part(const Part& p, __amdgpu_buffer_rsrc_t srd, int lane, unsigned so) {
#pragma unroll
  for (int k = 0; k < 4; ++k) __builtin_amdgcn_raw_buffer_store_b128(p.o[k], srd, lane * 16 + k * 1024, so, 16);
  __builtin_amdgcn_raw_buffer_store_b64(p.ml, srd, lane < 16 ? lane * 8 : PO_BYTES, so + 4096, 16);
}
__device__ __forceinline__ void request_parts(__amdgpu_buffer_rsrc_t srd, int lane, unsigned sa, unsigned sb) {
  asm volatile("s_nop 4\n\tbuffer_load_dwordx2 v[232:233], %1, %2, %4 offen sc1\n\tbuffer_load_dwordx2 v[250:251], %1, %2, %6 offen sc1\n\t"
               "buffer_load_dwordx4 v[216:219], %0, %2, %3 offen sc1\n\tbuffer_load_dwordx4 v[234:237], %0, %2, %5 offen sc1\n\t"
               "buffer_load_dwordx4 v[220:223], %0, %2, %3 offen offset:1024 sc1\n\tbuffer_load_dwordx4 v[238:241], %0, %2, %5 offen offset:1024 sc1\n\t"
               "buffer_load_dwordx4 v[224:227], %0, %2, %3 offen offset:2048 sc1\n\tbuffer_load_dwordx4 v[242:245], %0, %2, %5 offen offset:2048 sc1\n\t"
               "buffer_load_dwordx4 v[228:231], %0, %2, %3 offen offset:3072 sc1\n\tbuffer_load_dwordx4 v[246:249], %0, %2, %5 offen offset:3072 sc1"
               :: "v"(lane * 16), "v"((lane & 15) * 8), "s"(srd), "s"(sa), "s"(sa + 4096), "s"(sb), "s"(sb + 4096)
               : "memory", "v216", "v217", "v218", "v219", "v220", "v221", "v222", "v223", "v224", "v225", "v226", "v227", "v228", "v229", "v230", "v231", "v232", "v233",
                 "v234", "v235", "v236", "v237", "v238", "v239", "v240", "v241", "v242", "v243", "v244", "v245", "v246", "v247", "v248", "v249", "v250", "v251");
}
__device__ __forceinline__ void take_parts(Part& a, Part& b) {
  asm volatile("s_waitcnt vmcnt(0)" : "={v[216:219]}"(a.o[0]), "={v[220:223]}"(a.o[1]), "={v[224:227]}"(a.o[2]), "={v[228:231]}"(a.o[3]), "={v[232:233]}"(a.ml),
               "={v[234:237]}"(b.o[0]), "={v[238:241]}"(b.o[1]), "={v[242:245]}"(b.o[2]), "={v[246:249]}"(b.o[3]), "={v[250:251]}"(b.ml) :: "memory");
}
__device__ __forceinline__ void request_flag(GAS char* rec) { asm volatile("global_load_dwordx2 v[254:255], %0, off offset:8 sc1" :: "v"(rec) : "memory", "v254", "v255"); }
__device__ __forceinline__ u64 take_flag() {
  u64 f;
  asm volatile("s_waitcnt vmcnt(0)" : "={v[254:255]}"(f) :: "memory");
  return f;
}

struct Split { int n0, n, b2, t2, dep2, pages, wfix; };
__device__ __forceinline__ Split split_work(int len, int me, bool helper) {
  const int P = (len + 15) >> 4, rank = __builtin_popcount(less16<15>(P * 16 + (threadIdx.x & 15)));
  const int tot = rl(scan16(P), 15);
  const int rk = rl(rank, me), other = __builtin_ctz((unsigned)__builtin_amdgcn_ballot_w64(rank == 15 - rk));
  const bool isL = rk >= 8;
  const int Pm = rl(P, me), Po = rl(P, other), L = isL ? Pm : Po, S = isL ? Po : Pm, tL = (L + 3) >> 2, tS = (S + 3) >> 2;
  const int a2 = (L + S + 2 * XP + 4) >> 3;
  const int aS = min(a2 >> 1, tS - 1);
  int aL = a2 - (a2 >> 1);
  if (aS < (a2 >> 1)) aL = (int)(((unsigned)(L + S - 4 * aS + 2 * XP + 6) * 21846u) >> 18);
  aL = min(aL, tL - 1);
  const int R = L + S - 4 * (aL + aS);
  const int kL = max(aL, min(tL - 1, (2 * L - R + 4) >> 3));
  Split r;
  r.b2 = other; r.t2 = kL; r.dep2 = isL && kL > aL;
  if (!helper) { r.n0 = r.n = isL ? aL : aS; r.pages = 4 * r.n - XE; }
  else if (isL) { r.n0 = r.n = tL - kL; r.pages = L - 4 * kL; }
  else { r.n0 = tS - aS; r.n = r.n0 + kL - aL; r.pages = S - 4 * aS + 4 * (kL - aL); }
  const int share = (int)((float)(r.pages * 32 * 256) * __builtin_amdgcn_rcpf((float)max(tot - 16 * XE, 16)));
  r.wfix = __builtin_amdgcn_readfirstlane(min(max(2 * 256 + (W0 - 2) * share, 12 * 256), 48 * 256));
  return r;
}
__device__ __forceinline__ void load_q(bf16x8 (&qf)[4], const char* qrow, int b) {
#pragma unroll
  for (int s = 0; s < 4; ++s) qf[s] = *(const bf16x8*)(qrow + (size_t)b * (HQ * SQ * HD * 2) + s * 64);
}
__device__ __forceinline__ void zero_q(bf16x8 (&qf)[4]) {
#pragma unroll
  for (int s = 0; s < 4; ++s) qf[s] = __builtin_bit_cast(bf16x8, u32x4{0u, 0u, 0u, 0u});
}

__global__ __launch_bounds__(384) void paged_attn_kernel(const char* __restrict__ q, const char* __restrict__ kc, const char* __restrict__ vc,
                                                        const int* __restrict__ bt, const int* __restrict__ lens, char* __restrict__ out,
                                                        char* ws, unsigned kvbytes, unsigned pad_) {
  __shared__ __attribute__((aligned(1024))) char lds[LDS_BYTES];
  {
    const unsigned line = (blockIdx.x >> 3) * 6 + threadIdx.x;
    u64 pc0;
    asm volatile("s_getpc_b64 %0" : "=s"(pc0));
    if (threadIdx.x < 6) asm volatile("global_load_dword v252, %0, %1" :: "v"(line % (CODE_BYTES / 128) * 128), "s"(pc0 & ~255ull) : "memory", "v252");
  }
  const int bid = blockIdx.x, lane = threadIdx.x & 63, w = __builtin_amdgcn_readfirstlane(threadIdx.x >> 6);
  const bool helper = bid >= 128;
  const int h = bid & 7, me = (bid >> 3) & 15;
  const unsigned lds0 = (unsigned)(size_t)(LAS char*)lds;
  const unsigned g = lane >> 4, ch = lane & 15;
  lds_put(lds0 + DONE_OFF + 4 * w, 0);

  if (__builtin_expect(w == 4, 1)) {
    __builtin_amdgcn_s_setprio(3);
    u32x4 pg = *(const u32x4*)(bt + me * NBPS + 4 * lane);
    int len = lens[ch];
    __builtin_amdgcn_sched_barrier(0);
    lds_bar();
    const __amdgpu_buffer_rsrc_t srdK = __builtin_amdgcn_make_buffer_rsrc((void*)kc, (short)0, (int)kvbytes, 0x00020000);
    const __amdgpu_buffer_rsrc_t srdV = __builtin_amdgcn_make_buffer_rsrc((void*)vc, (short)0, (int)kvbytes, 0x00020000);
    u32x4 vk, vv;
#pragma unroll
    for (int i = 0; i < 4; ++i) {
      const unsigned row = 4 * i + g;
      vk[i] = row * 2048 + h * 256 + ((ch ^ row) << 4) - i * 1024;
      vv[i] = row * 2048 + h * 256 + ((ch ^ (2 * (row & 7))) << 4) - i * 1024;
    }
    asm volatile("" : "+v"(vk), "+v"(vv) : "s"(srdK), "s"(srdV));
    __builtin_amdgcn_sched_barrier(0);
    len = len <= 0 ? SKV : min(len, SKV);
    pg <<= 15;
    const int Pm = (rl(len, me) + 15) >> 4, tf = helper ? (Pm - 1) >> 2 : 0, cut0 = helper ? 4 * (Pm - 4 * tf) : 16;
    unsigned p0 = rl(pg[0], tf), p1 = rl(pg[1], tf), p2 = rl(pg[2], tf), p3 = rl(pg[3], tf);
    dma_page(lds0, vk, srdK, p0);
    if (cut0 > 4) dma_page(lds0 + 4096, vk, srdK, p1);
    if (cut0 > 8) dma_page(lds0 + 8192, vk, srdK, p2);
    if (cut0 > 12) dma_page(lds0 + 12288, vk, srdK, p3);
    dma_page(lds0 + VOFF, vv, srdV, p0);
    if (cut0 > 4) dma_page(lds0 + VOFF + 4096, vv, srdV, p1);
    if (cut0 > 8) dma_page(lds0 + VOFF + 8192, vv, srdV, p2);
    if (cut0 > 12) dma_page(lds0 + VOFF + 12288, vv, srdV, p3);
    __builtin_amdgcn_sched_barrier(0);
    for (int k = cut0; k < 16; ++k) *(LAS u32x4*)(size_t)(lds0 + VOFF + k * 1024 + lane * 16) = u32x4{0u, 0u, 0u, 0u};
    const Split sp = split_work(len, me, helper);
    const int n0 = sp.n0, n = sp.n;
    asm volatile("global_load_dwordx4 v[224:227], %0, off" :: "v"(bt + sp.b2 * NBPS + 4 * lane) : "memory", "v224", "v225", "v226", "v227");
    const unsigned fulla = lds0 + FULL_OFF, donea = lds0 + DONE_OFF;
    const int tend = helper ? n : n - (XE + 3) / 4;
    asm volatile("s_waitcnt lgkmcnt(0)" ::: "memory");
    const u64 tab = wait_tab();
    const int mark = 2 * cut0;
    int acc = 128, wadj = 0, issued = mark, rel = 0, hend = cut0, freed = 0, tl = tf;
    auto release = [&]() { hend += rel ? 16 : cut0; ++rel; lds_put(fulla, rel); };
    for (int t = 1; t < n; ++t) {
      acc = (acc & 255) + sp.wfix + wadj;
      const int win = t >= tend ? WEND : acc >> 8;
      const u64 ent = tab + 8 * (win - 4);
      tl += helper ? -1 : 1;
      if (t == n0) {
        wait_dyn(tab + 8 * min(issued - mark, 63));
        asm volatile("" : "={v[224:227]}"(pg));
        pg <<= 15;
        tl = sp.t2 - 1;
      }
      const unsigned dst = lds0 + (t & (NS - 1)) * SLOT;
      p0 = rl(pg[0], tl); p1 = rl(pg[1], tl); p2 = rl(pg[2], tl); p3 = rl(pg[3], tl);
      for (unsigned spins = 0; freed <= t - NS && spins < SPIN_CAP; ++spins) freed = lds_min4(donea);
#define CHUNK(dst, vo, srd, p) { wait_dyn(ent); dma_page(dst, vo, srd, p); if (issued - win + 4 >= hend) release(); issued += 4; }
      CHUNK(dst, vk, srdK, p0) CHUNK(dst + 4096, vk, srdK, p1) CHUNK(dst + 8192, vk, srdK, p2) CHUNK(dst + 12288, vk, srdK, p3)
      CHUNK(dst + VOFF, vv, srdV, p0) CHUNK(dst + VOFF + 4096, vv, srdV, p1) CHUNK(dst + VOFF + 8192, vv, srdV, p2)
      asm volatile("ds_read_b128 v[216:219], %0\n\tds_read_b32 v220, %0 offset:20" :: "v"(donea) : "memory", "v216", "v217", "v218", "v219", "v220");
      CHUNK(dst + VOFF + 12288, vv, srdV, p3)
#undef CHUNK
      u32x4 d4; int adj;
      asm volatile("s_waitcnt lgkmcnt(0)" : "={v[216:219]}"(d4), "={v220}"(adj) :: "memory");
      freed = __builtin_amdgcn_readfirstlane(min(min(d4[0], d4[1]), min(d4[2], d4[3])));
      wadj = __builtin_amdgcn_readfirstlane(min(max(adj, -4 * 256), 8 * 256));
    }
    while (rel < 2 * n) { wait_dyn(tab + 8 * min(issued - hend, 63)); release(); }
    asm volatile("s_waitcnt vmcnt(0)" ::: "memory");
    return;
  }

  const char* qrow = q + ((4 * h + w) * SQ + ch) * (HD * 2) + g * 16;
  bf16x8 qf[4], qn[4];
  lds_bar();
  __builtin_amdgcn_sched_barrier(0);
  int len = lens[ch];
  if (w < 4) load_q(qf, qrow, me);
  const unsigned unim = (unsigned)__builtin_amdgcn_ballot_w64(len <= 0);
  len = len <= 0 ? SKV : min(len, SKV);
  const Split sp = split_work(len, me, helper);
  const int n0 = sp.n0, n = sp.n;

  if (__builtin_expect(w == 5, 0)) {
    const int Pm = (rl(len, me) + 15) >> 4, cut0 = helper ? 4 * (((Pm - 1) & 3) + 1) : 16, steady = 8 * sp.pages;
    if (sp.pages < 48) return;
    auto reach = [&](int j) {
      for (unsigned spins = 0; spins < SPIN_CAP && lds_get(lds0 + FULL_OFF) < j; ++spins) __builtin_amdgcn_s_sleep(16);
      for (unsigned spins = 0; spins < SPIN_CAP && lds_get(lds0 + FULL_OFF) <= j; ++spins) __builtin_amdgcn_s_sleep(1);
      return now32();
    };
    const __amdgpu_buffer_rsrc_t srdG = __builtin_amdgcn_make_buffer_rsrc((void*)(ws + PO_OFF + PO_BYTES), (short)0, NROUND * NREF * PROG_STRIDE, 0x00020000);
    const int rk = bid / (256 / NREF), ref = bid == rk * (256 / NREF) + (rk * 11 + 2) % (256 / NREF) ? rk : -1;
    const unsigned tz = reach(3);
    float fprev = 0.f;
#pragma unroll 1
    for (int r = 0; r < NROUND; ++r) {
      const int goal = steady * (SF0 + r * SFD) / 100, j = (goal - 2 * cut0 + 8) / 16 + 1, hend = 2 * cut0 + 16 * (j - 1);
      const unsigned now = reach(j);
      const float tpl = (float)(int)(now - tz) * __builtin_amdgcn_rcpf((float)(16 * (j - 3)));
      const unsigned stamp = now + (int)((float)(goal - hend) * tpl);
      const unsigned po = r * NREF * PROG_STRIDE;
      if (ref >= 0) __builtin_amdgcn_raw_buffer_store_b128(u32x4{stamp, stamp, stamp, stamp}, srdG, lane * 16, po + ref * PROG_STRIDE, 16);
      for (unsigned spins = 0; spins < 4096 && (int)(now32() - stamp) < SDELAY; ++spins) __builtin_amdgcn_s_sleep(4);
      const int d = (int)(stamp - __builtin_amdgcn_raw_buffer_load_b32(srdG, (ch & (NREF - 1)) * PROG_STRIDE + (bid & 7) * 128, po, 16));
      const int tot = rl(scan16((unsigned)(d + 400) < 800u ? d * 1024 + 1 : 0), NREF - 1);
      const float since = (float)(int)(now32() - stamp) + 60.f;
      const float lag = (float)(tot >> 10) * __builtin_amdgcn_rcpf((float)max(tot & 1023, 1)) - fprev * since;
      const float span = r + 1 < NROUND ? tpl * (float)(steady * SFD / 100) : fmaxf(tpl * (float)(steady - goal) - since, 100.f);
      fprev = fminf(fmaxf(lag * __builtin_amdgcn_rcpf(span), -SCLAMP), SCLAMP);
      lds_put(lds0 + FULL_OFF + 4, (int)(SGAIN * (float)(sp.wfix - 512) * fprev));
    }
    return;
  }

  const unsigned slot0 = (unsigned)((me * 8 + h) * 2) * 4 + w, slot1 = (unsigned)(((helper ? sp.b2 : me) * 8 + h) * 2 + 1) * 4 + w;
  GAS char* rec = (GAS char*)ws + (lane & 1 ? slot1 : slot0) * REC_STRIDE;
  const bool two = helper ? n > n0 : sp.dep2, mine = !(lane & 1) || two;
  u64 tag = __hip_atomic_load((GAS u64*)rec, __ATOMIC_RELAXED, __HIP_MEMORY_SCOPE_AGENT) + TAG_STEP;
  if (n > n0 && !((unim >> sp.b2) & 1)) load_q(qn, qrow, sp.b2); else zero_q(qn);
  if ((unim >> me) & 1) zero_q(qf);
  const __amdgpu_buffer_rsrc_t srdP = __builtin_amdgcn_make_buffer_rsrc((void*)(ws + PO_OFF), (short)0, (int)PO_BYTES, 0x00020000);
  const __amdgpu_buffer_rsrc_t srdO = __builtin_amdgcn_make_buffer_rsrc((void*)out, (short)0, 16 * HQ * SQ * HD * 2, 0x00020000);
  const unsigned kaddr = lds0 + ch * 256 + ((g ^ ch) << 4);
  const unsigned vaddr = lds0 + VOFF + (4 * g + (ch >> 2)) * 256 + ((((lane >> 1) & 1) ^ (2 * ((4 * g + (ch >> 2)) & 7))) << 4) + 8 * (lane & 1);
  unsigned fulla = lds0 + FULL_OFF, donea = lds0 + DONE_OFF + 4 * w;
  int nv = helper ? ((rl(len, me) - 1) & 63) + 1 : 64;
  bf16x8 ones = __builtin_bit_cast(bf16x8, u32x4{0x3f803f80u, 0x3f803f80u, 0x3f803f80u, 0x3f803f80u});
  f32x4 o[8], l = {0.f, 0.f, 0.f, 0.f};
  float m = -__builtin_inff();
#pragma unroll
  for (int k = 0; k < 8; ++k) o[k] = f32x4{0.f, 0.f, 0.f, 0.f};
  Part held = {};
  asm volatile("" : "+v"(ones), "+v"(fulla), "+v"(donea), "+v"(tag), "+v"(qf[0]), "+v"(qf[1]), "+v"(qf[2]), "+v"(qf[3]), "+v"(qn[0]), "+v"(qn[1]), "+v"(qn[2]), "+v"(qn[3])
               : "s"(srdP), "s"(srdO));
  int full = 0, pst = 0;
  auto acquire = [&](int j) {
    for (unsigned spins = 0; full <= j && spins < SPIN_CAP; ++spins) {
      full = lds_get(fulla);
      if (full <= j) __builtin_amdgcn_s_sleep(1);
    }
    asm volatile("" ::: "memory");
  };
  auto poll = [&]() {
    if (pst == 1 && __builtin_amdgcn_ballot_w64(take_flag() != tag && mine) == 0) {
      request_parts(srdP, lane, slot0 * PO_WAVE, two ? slot1 * PO_WAVE : PO_BYTES);
      pst = 2;
    }
    if (pst < 2) { request_flag(rec); pst = 1; }
  };
  const int ipoll = helper ? -1 : n - 1;
  for (int i = 0; i < n; ++i) {
    if (__builtin_expect(i == n0, 0)) {
      pack_part(held, o, m, l[0]);
#pragma unroll
      for (int k = 0; k < 8; ++k) o[k] = f32x4{0.f, 0.f, 0.f, 0.f};
      l = f32x4{0.f, 0.f, 0.f, 0.f};
      m = -__builtin_inff();
#pragma unroll
      for (int s = 0; s < 4; ++s) qf[s] = qn[s];
    }
    acquire(2 * i);
    if (__builtin_expect(i == ipoll, 0)) poll();
    const unsigned so = (i & (NS - 1)) * SLOT;
    f32x4 s[4];
    {
      bf16x8 kf[4][4];
#pragma unroll
      for (int st = 0; st < 4; ++st)
#pragma unroll
        for (int kb = 0; kb < 4; ++kb) kf[kb][st] = *(LAS bf16x8*)(size_t)(((kaddr + so) ^ (st << 6)) + kb * 4096);
#pragma unroll
      for (int kb = 0; kb < 4; ++kb) s[kb] = f32x4{0.f, 0.f, 0.f, 0.f};
#pragma unroll
      for (int st = 0; st < 4; ++st)
#pragma unroll
        for (int kb = 0; kb < 4; ++kb) s[kb] = __builtin_amdgcn_mfma_f32_16x16x32_bf16(kf[kb][st], qf[st], s[kb], 0, 0, 0);
      __builtin_amdgcn_sched_group_barrier(0x2, 5, 1);
      __builtin_amdgcn_sched_group_barrier(0x100, 6, 1);
#pragma unroll
      for (int k = 0; k < 10; ++k) { __builtin_amdgcn_sched_group_barrier(0x8, 1, 1); __builtin_amdgcn_sched_group_barrier(0x100, 1, 1); }
      __builtin_amdgcn_sched_group_barrier(0x8, 6, 1);
    }
    if (__builtin_expect(nv < 64, 0)) {
      const int lim = nv - 4 * g;
#pragma unroll
      for (int kb = 0; kb < 4; ++kb)
#pragma unroll
        for (int r = 0; r < 4; ++r) s[kb][r] = (16 * kb + r < lim) ? s[kb][r] : -__builtin_inff();
      nv = 64;
    }
    const float t0 = MX(MX(MX(MX(s[0][0], s[0][1]), s[0][2]), s[0][3]), s[1][0]), t1 = MX(MX(MX(MX(s[1][1], s[1][2]), s[1][3]), s[2][0]), s[2][1]);
    const float t2 = MX(MX(MX(MX(s[2][2], s[2][3]), s[3][0]), s[3][1]), s[3][2]);
    const float t = MX(MX(MX(t0, t1), t2), s[3][3]);
    if (__builtin_expect(__builtin_amdgcn_ballot_w64(!(fmaf(t, CSCALE, -m) <= THR)) != 0, 0)) {
      const float mn = MX(m, xmax(t) * CSCALE), alpha = __builtin_amdgcn_exp2f(m - mn);
#pragma unroll
      for (int k = 0; k < 8; ++k) o[k] *= alpha;
      l *= alpha;
      m = mn;
    }
    bf16x8 pk[2];
#pragma unroll
    for (int kb = 0; kb < 4; ++kb)
#pragma unroll
      for (int r = 0; r < 4; ++r) s[kb][r] = __builtin_amdgcn_exp2f(fmaf(s[kb][r], CSCALE, -m));
#pragma unroll
    for (int u = 0; u < 2; ++u)
      pk[u] = __builtin_bit_cast(bf16x8, u32x4{pk2(s[2 * u][0], s[2 * u][1]), pk2(s[2 * u][2], s[2 * u][3]), pk2(s[2 * u + 1][0], s[2 * u + 1][1]), pk2(s[2 * u + 1][2], s[2 * u + 1][3])});
    l = __builtin_amdgcn_mfma_f32_16x16x32_bf16(ones, pk[0], l, 0, 0, 0);
    l = __builtin_amdgcn_mfma_f32_16x16x32_bf16(ones, pk[1], l, 0, 0, 0);
    asm volatile("" : "+v"(pk[0]), "+v"(pk[1]));
    acquire(2 * i + 1);
    if (__builtin_expect(i == ipoll, 0)) poll();
    {
      bf16x8 vf[8][2];
#pragma unroll
      for (int k = 0; k < 8; ++k) {
        const unsigned a = (vaddr + so) ^ (k << 5);
#pragma unroll
        for (int u = 0; u < 2; ++u) {
          const bf16x4 lo = __builtin_amdgcn_ds_read_tr16_b64_v4bf16((LAS bf16x4*)(size_t)(a + u * 8192));
          const bf16x4 hi = __builtin_amdgcn_ds_read_tr16_b64_v4bf16((LAS bf16x4*)(size_t)(a + u * 8192 + 4096));
          vf[k][u] = __builtin_shufflevector(lo, hi, 0, 1, 2, 3, 4, 5, 6, 7);
        }
      }
#pragma unroll
      for (int k = 0; k < 8; ++k)
#pragma unroll
        for (int u = 0; u < 2; ++u) o[k] = __builtin_amdgcn_mfma_f32_16x16x32_bf16(vf[k][u], pk[u], o[k], 0, 0, 0);
      __builtin_amdgcn_sched_group_barrier(0x2, 8, 0);
      __builtin_amdgcn_sched_group_barrier(0x100, 12, 0);
#pragma unroll
      for (int k = 0; k < 10; ++k) { __builtin_amdgcn_sched_group_barrier(0x8, 1, 0); __builtin_amdgcn_sched_group_barrier(0x100, 2, 0); }
      __builtin_amdgcn_sched_group_barrier(0x8, 6, 0);
    }
    asm volatile("s_waitcnt lgkmcnt(0)\n\tds_write_b32 %0, %1" :: "v"(donea), "v"(i + 1) : "memory");
  }
  if (helper) {
    if (two) store_part(held, srdP, lane, slot0 * PO_WAVE);
    pack_part(held, o, m, l[0]);
    store_part(held, srdP, lane, (two ? slot1 : slot0) * PO_WAVE);
    asm volatile("s_waitcnt vmcnt(0)" ::: "memory");
    if (lane < 2 && mine) __hip_atomic_store((GAS u64*)(rec + 8), tag, __ATOMIC_RELAXED, __HIP_MEMORY_SCOPE_AGENT);
    return;
  }
  for (unsigned spins = 0; pst < 2 && spins < SPIN_CAP; ++spins) poll();
  Part pa, pb;
  take_parts(pa, pb);
  {
    const float m0 = __uint_as_float(pa.ml[0]), m1 = two ? __uint_as_float(pb.ml[0]) : -__builtin_inff();
    const float mn = MX(MX(m, m0), m1);
    float a0 = __builtin_amdgcn_exp2f(m - mn), w0 = __builtin_amdgcn_exp2f(m0 - mn), w1 = __builtin_amdgcn_exp2f(m1 - mn);
    const float inv = __builtin_amdgcn_rcpf(l[0] * a0 + w0 * __uint_as_float(pa.ml[1]) + w1 * __uint_as_float(pb.ml[1]));
    a0 *= inv; w0 *= inv; w1 *= inv;
    const unsigned oo = (((me * HQ + 4 * h + w) * SQ + ch) * HD + 16 * (g & 1) + 8 * (g >> 1)) * 2;
#pragma unroll
    for (int k = 0; k < 8; k += 2) {
      const f32x4 x = o[k] * a0 + w0 * part_o(pa, k) + w1 * part_o(pb, k), y = o[k + 1] * a0 + w0 * part_o(pa, k + 1) + w1 * part_o(pb, k + 1);
      auto r0 = __builtin_amdgcn_permlane16_swap(pk2(x[0], x[1]), pk2(y[0], y[1]), false, false);
      auto r1 = __builtin_amdgcn_permlane16_swap(pk2(x[2], x[3]), pk2(y[2], y[3]), false, false);
      __builtin_amdgcn_raw_buffer_store_b128(u32x4{r0[0], r1[0], r0[1], r1[1]}, srdO, oo + k * 32, 0, 18);
    }
  }
  if (lane < 2 && mine) __hip_atomic_store((GAS u64*)rec, tag, __ATOMIC_RELAXED, __HIP_MEMORY_SCOPE_AGENT);
}

}

extern "C" void kernel_launch(void* const* d_in, const int* in_sizes, int n_in, void* d_out, int out_size, void* d_ws, size_t ws_size,
                              hipStream_t stream) {
  (void)n_in; (void)out_size; (void)ws_size;
  hipLaunchKernelGGL(paged_attn_kernel, dim3(256), dim3(384), 0, stream, (const char*)d_in[0], (const char*)d_in[1], (const char*)d_in[2],
                     (const int*)d_in[3], (const int*)d_in[4], (char*)d_out, (char*)d_ws, (unsigned)in_sizes[1] * 2u, 0u);
}
```
